```python
import torch

def _softmax(x, axis=-1):
    return torch.softmax(x, dim=axis)
def _gelu(x):
    return torch.nn.functional.gelu(x, approximate="tanh")
def _silu(x):
    return torch.nn.functional.silu(x)
def _sigmoid(x):
    return torch.sigmoid(x)
def _layernorm(x, axis=-1, eps=1e-5):
    m = x.mean(dim=axis, keepdim=True)
    v = x.var(dim=axis, keepdim=True, unbiased=False)
    return (x - m) / torch.sqrt(v + eps)
def _scatter_add(tgt, idx, src, axis=0):
    return tgt.clone().index_add_(axis, idx, src)
def _segment_sum(src, seg, n):
    out = torch.zeros((n,) + src.shape[1:], dtype=src.dtype)
    return out.index_add_(0, seg, src)
def _pad(x, pw):
    flat = tuple(v for pair in reversed(pw) for v in pair)
    return torch.nn.functional.pad(x, flat)

kernel_name = 'concat_heads__mha_block_cached__gqa_block_cached__near_ridge'

def setup_inputs():

    _w = (lambda a: (lambda t: t.to(torch.bfloat16) if t.is_floating_point() else t)(torch.as_tensor(a)))
    def _A_setup():
        B, Sq, D = 8, 112, 1024
        H, Sk, Dh = 32, 1024, 128
        return {
            "x": _w((lambda *s: torch.randn(*s, dtype=torch.bfloat16) * 0.8)(B, Sq, D)),
            "k_cache": _w((lambda *s: torch.randn(*s, dtype=torch.bfloat16) * 0.8)(B, H, Sk, Dh)),
            "v_cache": _w((lambda *s: torch.randn(*s, dtype=torch.bfloat16) * 0.05)(B, H, Sk, Dh)),
            "wq": _w((lambda *s: torch.randn(*s, dtype=torch.bfloat16) * 0.8)(D, H * Dh) * (D ** -0.5)),
            "wo": _w((lambda *s: torch.randn(*s, dtype=torch.bfloat16) * 0.05)(H * Dh, D) * ((H * Dh) ** -0.5)),
        }
    _A_in = _A_setup()

    def _B_setup():
        B, Sq, D = 8, 112, 1024
        Hq, Hkv, Sk, Dh = 32, 32, 1024, 128
        assert Hq % Hkv == 0, (Hq, Hkv)
        return {
            "x": _w((lambda *s: torch.randn(*s, dtype=torch.bfloat16) * 0.8)(B, Sq, D)),
            "k_cache": _w((lambda *s: torch.randn(*s, dtype=torch.bfloat16) * 0.8)(B, Hkv, Sk, Dh)),
            "v_cache": _w((lambda *s: torch.randn(*s, dtype=torch.bfloat16) * 0.05)(B, Hkv, Sk, Dh)),
            "wq": _w((lambda *s: torch.randn(*s, dtype=torch.bfloat16) * 0.8)(D, Hq * Dh) * (D ** -0.5)),
            "wo": _w((lambda *s: torch.randn(*s, dtype=torch.bfloat16) * 0.05)(Hq * Dh, D) * ((Hq * Dh) ** -0.5)),
        }
    _B_in = _B_setup()

    return {
        "A_x": _A_in["x"],
        "A_k_cache": _A_in["k_cache"],
        "A_v_cache": _A_in["v_cache"],
        "A_wq": _A_in["wq"],
        "A_wo": _A_in["wo"],
        "B_x": _B_in["x"],
        "B_k_cache": _B_in["k_cache"],
        "B_v_cache": _B_in["v_cache"],
        "B_wq": _B_in["wq"],
        "B_wo": _B_in["wo"],
    }

def reference(A_x, A_k_cache, A_v_cache, A_wq, A_wo, B_x, B_k_cache, B_v_cache, B_wq, B_wo):
    def _A_ref(x, k_cache, v_cache, wq, wo):
        B, Sq, D = x.shape
        H, Dh = 32, 128
        q = (lambda x, *d: x.permute(*d))((x @ wq).reshape(B, Sq, H, Dh), 0, 2, 1, 3)
        scores = torch.einsum("bhqd,bhkd->bhqk", q, k_cache) * (Dh ** -0.5)
        p = _softmax(scores, axis=-1)
        o = torch.einsum("bhqk,bhkd->bhqd", p, v_cache)
        return (lambda x, *d: x.permute(*d))(o, 0, 2, 1, 3).reshape(B, Sq, H * Dh) @ wo

    def _B_ref(x, k_cache, v_cache, wq, wo):
        B, Sq, D = x.shape
        Hq, Dh = 32, 128
        Hkv = k_cache.shape[1]
        rep = Hq // Hkv
        q = (lambda x, *d: x.permute(*d))((x @ wq).reshape(B, Sq, Hq, Dh), 0, 2, 1, 3)
        k = (lambda x, n, axis: torch.repeat_interleave(x, n, dim=axis))(k_cache, rep, axis=1)
        v = (lambda x, n, axis: torch.repeat_interleave(x, n, dim=axis))(v_cache, rep, axis=1)
        scores = torch.einsum("bhqd,bhkd->bhqk", q, k) * (Dh ** -0.5)
        p = _softmax(scores, axis=-1)
        o = torch.einsum("bhqk,bhkd->bhqd", p, v)
        return (lambda x, *d: x.permute(*d))(o, 0, 2, 1, 3).reshape(B, Sq, Hq * Dh) @ wo

    a = _A_ref(A_x, A_k_cache, A_v_cache, A_wq, A_wo)
    b = _B_ref(B_x, B_k_cache, B_v_cache, B_wq, B_wo)
    return torch.concatenate([a, b], axis=-1)
```

```cpp
#include <hip/hip_runtime.h>
#include <hip/hip_bf16.h>
typedef unsigned short u16;
__device__ __forceinline__ float bf2f(u16 x){ return __uint_as_float(((unsigned)x)<<16); }
__device__ __forceinline__ u16 f2bf(float f){ unsigned u=__float_as_uint(f); u += 0x7fff + ((u>>16)&1); return (u16)(u>>16); }
constexpr int B=8,SQ=112,D=1024,H=32,SK=1024,DH=128;
__global__ void gemm_naive(const u16* A, const u16* W, u16* C, int M, int N, int K, int ldc){
  __shared__ float As[16][17], Ws[16][17];
  int tx=threadIdx.x&15, ty=threadIdx.x>>4;
  int row=blockIdx.y*16+ty, col=blockIdx.x*16+tx;
  float acc=0;
  for(int k0=0;k0<K;k0+=16){
    As[ty][tx]=bf2f(A[(size_t)row*K+k0+tx]);
    Ws[ty][tx]=bf2f(W[(size_t)(k0+ty)*N+col]);
    __syncthreads();
    for(int k=0;k<16;k++) acc+=As[ty][k]*Ws[k][tx];
    __syncthreads();
  }
  C[(size_t)row*ldc+col]=f2bf(acc);
}
__global__ void attn_naive(const u16* Q, const u16* K, const u16* V, u16* O){
  int q=blockIdx.x, h=blockIdx.y, b=blockIdx.z, t=threadIdx.x;
  __shared__ float qs[DH], s[SK], red[128];
  qs[t]=bf2f(Q[((size_t)(b*SQ+q))*(H*DH)+h*DH+t]);
  __syncthreads();
  const u16* Kb=K+((size_t)(b*H+h))*SK*DH; const u16* Vb=V+((size_t)(b*H+h))*SK*DH;
  float m=-1e30f;
  for(int k=t;k<SK;k+=128){ float a=0; for(int d=0;d<DH;d++) a+=qs[d]*bf2f(Kb[(size_t)k*DH+d]); a*=0.08838834764831845f; s[k]=a; m=fmaxf(m,a);}
  red[t]=m; __syncthreads();
  for(int o=64;o>0;o>>=1){ if(t<o) red[t]=fmaxf(red[t],red[t+o]); __syncthreads(); }
  m=red[0]; __syncthreads();
  float l=0; for(int k=t;k<SK;k+=128){ float p=__expf(s[k]-m); s[k]=p; l+=p; }
  red[t]=l; __syncthreads();
  for(int o=64;o>0;o>>=1){ if(t<o) red[t]+=red[t+o]; __syncthreads(); }
  l=red[0];
  float acc=0; for(int k=0;k<SK;k++) acc+=s[k]*bf2f(Vb[(size_t)k*DH+t]);
  O[((size_t)(b*SQ+q))*(H*DH)+h*DH+t]=f2bf(acc/l);
}
extern "C" void kernel_launch(void* const* d_in, const int* in_sizes, int n_in, void* d_out, int out_size, void* d_ws, size_t ws_size, hipStream_t stream){
  for(int br=0;br<2;br++){
    const u16* x=(const u16*)d_in[br*5+0]; const u16* k=(const u16*)d_in[br*5+1]; const u16* v=(const u16*)d_in[br*5+2];
    const u16* wq=(const u16*)d_in[br*5+3]; const u16* wo=(const u16*)d_in[br*5+4];
    u16* q=(u16*)d_ws + (size_t)br*2*896*4096; u16* o=q+(size_t)896*4096;
    gemm_naive<<<dim3(4096/16,896/16),256,0,stream>>>(x,wq,q,896,4096,1024,4096);
    attn_naive<<<dim3(SQ,H,B),128,0,stream>>>(q,k,v,o);
    gemm_naive<<<dim3(1024/16,896/16),256,0,stream>>>(o,wo,(u16*)d_out+br*1024,896,1024,4096,2048);
  }
}
```

```cpp
#include <hip/hip_runtime.h>
#include <stdint.h>

typedef unsigned short u16;
typedef __bf16 bf16x8 __attribute__((ext_vector_type(8)));
typedef __bf16 bf16x4 __attribute__((ext_vector_type(4)));
typedef __bf16 bf16x2 __attribute__((ext_vector_type(2)));
typedef float f32x16 __attribute__((ext_vector_type(16)));
typedef float f32x4 __attribute__((ext_vector_type(4)));
typedef float f32x2 __attribute__((ext_vector_type(2)));
typedef unsigned u32x4 __attribute__((ext_vector_type(4)));
#define LAS __attribute__((address_space(3)))
#define GAS __attribute__((address_space(1)))
#define DI __device__ __forceinline__
typedef __amdgpu_buffer_rsrc_t rsrc_t;

constexpr int SQ = 112, DM = 1024, NH = 32, SK = 1024, DH = 128;
constexpr size_t WS_O = 0;
constexpr size_t WS_P = (size_t)16 << 20;
constexpr size_t WS_ST = (size_t)64 << 20;
constexpr float QSCALE = 0.08838834764831845f * 1.4426950408889634f;
constexpr float THR = 8.0f;

#define KV_NT 1
#define W_NT 0
#define X_NT 0
#define NSLOT 4
#define LOOKA 2

#define CSTAMP(ws, kern, idx) do {} while (0)
#define STAMP(ws, kern, idx) do {} while (0)

DI rsrc_t mkrsrc(const void* p, unsigned bytes) { return __builtin_amdgcn_make_buffer_rsrc((void*)p, (short)0, (int)bytes, 0x00020000); }
template <int NT> DI void dma16(unsigned m0v, unsigned voff, rsrc_t srd, unsigned soff) {
  if constexpr (NT) asm volatile("s_mov_b32 m0, %0\n\ts_nop 0\n\tbuffer_load_dwordx4 %1, %2, %3 offen nt lds" ::"s"(m0v), "v"(voff), "s"(srd), "s"(soff) : "memory");
  else              asm volatile("s_mov_b32 m0, %0\n\ts_nop 0\n\tbuffer_load_dwordx4 %1, %2, %3 offen lds" ::"s"(m0v), "v"(voff), "s"(srd), "s"(soff) : "memory");
}
template <int N> DI void wait_bar() { asm volatile("s_waitcnt vmcnt(%0) lgkmcnt(0)\n\ts_barrier" ::"n"(N) : "memory"); }
DI void bar() { asm volatile("s_waitcnt lgkmcnt(0)\n\ts_barrier" ::: "memory"); }
DI bf16x8 lds128(unsigned a) { return *(const LAS bf16x8*)(size_t)a; }
DI bf16x4 ldstr(unsigned a) { return __builtin_amdgcn_ds_read_tr16_b64_v4bf16((LAS bf16x4*)(size_t)a); }
DI bf16x8 cat(bf16x4 a, bf16x4 b) { return __builtin_shufflevector(a, b, 0, 1, 2, 3, 4, 5, 6, 7); }
DI f32x16 mfma(bf16x8 a, bf16x8 b, f32x16 c) { return __builtin_amdgcn_mfma_f32_32x32x16_bf16(a, b, c, 0, 0, 0); }
DI unsigned cvtpk(float lo, float hi) { f32x2 v = {lo, hi}; return __builtin_bit_cast(unsigned, __builtin_convertvector(v, bf16x2)); }
DI bf16x8 pack8(const f32x16& a, int o, float s) {
  u32x4 w = {cvtpk(a[o] * s, a[o + 1] * s), cvtpk(a[o + 2] * s, a[o + 3] * s), cvtpk(a[o + 4] * s, a[o + 5] * s), cvtpk(a[o + 6] * s, a[o + 7] * s)};
  return __builtin_bit_cast(bf16x8, w);
}
DI bf16x8 pack8n(const f32x16& a, int o) {
  u32x4 w = {cvtpk(a[o], a[o + 1]), cvtpk(a[o + 2], a[o + 3]), cvtpk(a[o + 4], a[o + 5]), cvtpk(a[o + 6], a[o + 7])};
  return __builtin_bit_cast(bf16x8, w);
}
DI float xhalf_max(float x) { auto r = __builtin_amdgcn_permlane32_swap(__float_as_uint(x), __float_as_uint(x), false, false); return fmaxf(__uint_as_float(r[0]), __uint_as_float(r[1])); }
DI float xhalf_sum(float x) { auto r = __builtin_amdgcn_permlane32_swap(__float_as_uint(x), __float_as_uint(x), false, false); return __uint_as_float(r[0]) + __uint_as_float(r[1]); }

DI void wstep(f32x16 (&acc)[4], unsigned wa  , bf16x8 b0, bf16x8 b1) {
#pragma unroll
  for (int c = 0; c < 4; ++c) { const unsigned a = wa ^ (c << 6); acc[c] = mfma(cat(ldstr(a), ldstr(a + 1024)), b0, acc[c]); }
#pragma unroll
  for (int c = 0; c < 4; ++c) { const unsigned a = wa ^ (c << 6); acc[c] = mfma(cat(ldstr(a + 4096), ldstr(a + 5120)), b1, acc[c]); }
}

constexpr unsigned LDS_X = 65536, LDS_W = 131072;

__global__ __launch_bounds__(512, 2)
void k_attn(const u16* __restrict__ xA, const u16* __restrict__ kA, const u16* __restrict__ vA, const u16* __restrict__ wqA,
            const u16* __restrict__ xB, const u16* __restrict__ kB, const u16* __restrict__ vB, const u16* __restrict__ wqB, char* __restrict__ ws) {
  extern __shared__ __attribute__((aligned(16))) char smem[];
  const unsigned lds0 = (unsigned)(size_t)(LAS char*)smem;
  const unsigned lane = threadIdx.x & 63, wid = __builtin_amdgcn_readfirstlane(threadIdx.x >> 6);
  const unsigned bid = blockIdx.x, xcd = bid & 7, rr = bid >> 3, br = xcd & 1, g = xcd >> 1, hh = rr & 7, p = rr >> 3;
  const unsigned head = 8 * g + hh, it = wid >> 2, w4 = wid & 3, batch = 2 * p + it;
  STAMP(ws, 0, 0); CSTAMP(ws, 0, 4);
  const rsrc_t srdX = mkrsrc((const char*)(br ? xB : xA) + (size_t)batch * SQ * DM * 2, SQ * DM * 2);
  const rsrc_t srdW = mkrsrc((const char*)(br ? wqB : wqA) + head * 256, DM * NH * DH * 2 - head * 256);
  const rsrc_t srdK = mkrsrc((const char*)(br ? kB : kA) + (size_t)(batch * NH + head) * SK * DH * 2, SK * DH * 2);
  const rsrc_t srdV = mkrsrc((const char*)(br ? vB : vA) + (size_t)(batch * NH + head) * SK * DH * 2, SK * DH * 2);
  const unsigned r32 = lane & 31, h = lane >> 5, l4 = lane >> 4, l15 = lane & 15;
  const unsigned swzV = (l15 ^ (l4 << 2)) << 4;
  const unsigned voffW = l4 * 8192 + swzV;
  const unsigned voffV = l4 * 256 + swzV;
  const unsigned voffK = l4 * 256 + ((l15 ^ ((w4 << 2) | l4)) << 4);
  const unsigned voffX0 = (32 * w4 + (lane >> 3)) * 2048 + (((lane & 7) ^ l4) << 4);
  const unsigned rot = (2 * hh + 4 * p) & 15;
  auto issue_x = [&](unsigned v) {
    const unsigned so = ((v + rot) & 15) * 128, m = lds0 + LDS_X + wid * 8192 + (v & 1) * 4096;
    dma16<X_NT>(m, voffX0, srdX, so);
    dma16<X_NT>(m + 1024, (voffX0 ^ 64) + 8 * 2048, srdX, so);
    dma16<X_NT>(m + 2048, voffX0 + 16 * 2048, srdX, so);
    dma16<X_NT>(m + 3072, (voffX0 ^ 64) + 24 * 2048, srdX, so);
  };
  auto issue_w = [&](unsigned u) {
    const unsigned k0 = (((u >> 1) + rot) & 15) * 64 + (u & 1) * 32 + 4 * wid;
    dma16<W_NT>(lds0 + LDS_W + (u & 3) * 8192 + wid * 1024, voffW, srdW, k0 * 8192);
  };
  auto issue_kv = [&](unsigned t, unsigned slot) {
    const unsigned m = lds0 + slot * 32768 + it * 16384 + w4 * 1024, so = t * 8192 + w4 * 1024;
    dma16<KV_NT>(m, voffK, srdK, so);
    dma16<KV_NT>(m + 4096, voffK, srdK, so + 4096);
    dma16<KV_NT>(m + 8192, voffV, srdV, so);
    dma16<KV_NT>(m + 12288, voffV, srdV, so + 4096);
  };
  const unsigned q4 = (lane >> 2) & 3, pp = lane & 3, blk = l4 & 1;
  const unsigned trl = q4 * 256 + (q4 << 6) + (2 * blk + (pp & 1)) * 16 + (pp >> 1) * 8;

  f32x16 qa[4] = {};
  {
    issue_x(0); issue_w(0); issue_w(1); issue_x(1); issue_w(2);
    const unsigned wbase = lds0 + LDS_W + trl + h * 2048;
    const unsigned xbase = lds0 + LDS_X + wid * 8192 + r32 * 128 + ((h ^ ((r32 >> 1) & 7)) << 4);
    bf16x8 xf[4];
#pragma unroll 1
    for (unsigned I = 0; I < 7; ++I) {
#pragma unroll
      for (unsigned uu = 0; uu < 4; ++uu) {
        const unsigned u = 4 * I + uu;
        if (uu & 1) wait_bar<10>(); else wait_bar<6>();
        if (!(uu & 1)) {
#pragma unroll
          for (int s = 0; s < 4; ++s) xf[s] = lds128((xbase + (uu >> 1) * 4096) ^ (s << 5));
        }
        wstep(qa, wbase + uu * 8192, xf[2 * (uu & 1)], xf[2 * (uu & 1) + 1]);
        asm volatile("s_waitcnt lgkmcnt(0)" ::: "memory");
        issue_w(u + 3);
        if (!(uu & 1)) issue_x((u >> 1) + 2);
      }
    }
    wait_bar<6>();
#pragma unroll
    for (int s = 0; s < 4; ++s) xf[s] = lds128(xbase ^ (s << 5));
    wstep(qa, wbase, xf[0], xf[1]);
    issue_w(31);
    wait_bar<6>();
    wstep(qa, wbase + 8192, xf[2], xf[3]);
    issue_kv(0, 0);
    wait_bar<5>();
#pragma unroll
    for (int s = 0; s < 4; ++s) xf[s] = lds128((xbase + 4096) ^ (s << 5));
    wstep(qa, wbase + 2 * 8192, xf[0], xf[1]);
    issue_kv(1, 1);
    wait_bar<8>();
    wstep(qa, wbase + 3 * 8192, xf[2], xf[3]);
  }
  bf16x8 qf[8];
#pragma unroll
  for (int c = 0; c < 4; ++c) { qf[2 * c] = pack8(qa[c], 0, QSCALE); qf[2 * c + 1] = pack8(qa[c], 8, QSCALE); }
  STAMP(ws, 0, 1); CSTAMP(ws, 0, 5);

  f32x16 o[4] = {};
  f32x16 negm = {};
  float mref = 0.f, lsum = 0.f;
  const unsigned kbase = lds0 + it * 16384 + r32 * 256 + (((h ^ r32) & 15) << 4);
  const unsigned vbase = lds0 + it * 16384 + 8192 + trl + h * 1024;
  unsigned slot = 0, slotn = LOOKA % NSLOT;
  if (LOOKA == 3) { bar(); issue_kv(2, 2); }
#pragma unroll 1
  for (unsigned t = 0; t < 32; ++t) {
    if (t + LOOKA <= 32) wait_bar<4 * (LOOKA - 1)>(); else if (LOOKA == 3 && t == 30) wait_bar<4>(); else wait_bar<0>();
    const unsigned ka = kbase + slot * 32768, va = vbase + slot * 32768;
    f32x16 s = mfma(lds128(ka), qf[0], negm);
#pragma unroll
    for (int i = 1; i < 8; ++i) s = mfma(lds128(ka ^ (i << 5)), qf[i], s);
    float mx = fmaxf(fmaxf(s[0], s[1]), s[2]);
#pragma unroll
    for (int i = 3; i < 15; i += 2) mx = fmaxf(fmaxf(mx, s[i]), s[i + 1]);
    mx = xhalf_max(fmaxf(mx, s[15]));
    if (__builtin_expect(t == 0 || __any(mx > THR), 0)) {
      const float d = t == 0 ? mx : fmaxf(mx, 0.f), f = t == 0 ? 1.f : __builtin_amdgcn_exp2f(-d);
      mref += d; lsum *= f;
#pragma unroll
      for (int i = 0; i < 16; ++i) { s[i] -= d; negm[i] = -mref; }
#pragma unroll
      for (int c = 0; c < 4; ++c)
#pragma unroll
        for (int i = 0; i < 16; ++i) o[c][i] *= f;
    }
    float l0 = 0.f, l1 = 0.f;
#pragma unroll
    for (int i = 0; i < 16; i += 2) { s[i] = __builtin_amdgcn_exp2f(s[i]); s[i + 1] = __builtin_amdgcn_exp2f(s[i + 1]); l0 += s[i]; l1 += s[i + 1]; }
    lsum += l0 + l1;
    const bf16x8 p0 = pack8n(s, 0), p1 = pack8n(s, 8);
#pragma unroll
    for (int c = 0; c < 4; ++c) { const unsigned a = va ^ (c << 6); o[c] = mfma(cat(ldstr(a), ldstr(a + 2048)), p0, o[c]); }
#pragma unroll
    for (int c = 0; c < 4; ++c) { const unsigned a = va ^ (c << 6); o[c] = mfma(cat(ldstr(a + 4096), ldstr(a + 6144)), p1, o[c]); }
    if (t + LOOKA < 32) issue_kv(t + LOOKA, slotn);
    slot = slot + 1 == NSLOT ? 0 : slot + 1;
    slotn = slotn + 1 == NSLOT ? 0 : slotn + 1;
  }
  STAMP(ws, 0, 2); CSTAMP(ws, 0, 6);
  const float inv = 1.f / xhalf_sum(lsum);
  const unsigned jb = w4 < 3 ? it * 3 + w4 : 6, col = w4 < 3 ? r32 : it * 16 + r32;
  char* op = ws + WS_O + ((size_t)(((br * 4 + p) * 32 + head) * 7 + jb) * 8) * 1024 + (h * 32 + col) * 16;
  if (w4 < 3 || r32 < 16) {
#pragma unroll
    for (int c = 0; c < 4; ++c) {
      *(GAS bf16x8*)(op + (2 * c) * 1024) = pack8(o[c], 0, inv);
      *(GAS bf16x8*)(op + (2 * c + 1) * 1024) = pack8(o[c], 8, inv);
    }
  }
  STAMP(ws, 0, 3);
}

__global__ __launch_bounds__(512, 2)
void k_oproj(const u16* __restrict__ woA, const u16* __restrict__ woB, char* __restrict__ ws) {
  extern __shared__ __attribute__((aligned(16))) char smem[];
  const unsigned lds0 = (unsigned)(size_t)(LAS char*)smem;
  const unsigned lane = threadIdx.x & 63, wid = __builtin_amdgcn_readfirstlane(threadIdx.x >> 6);
  const unsigned bid = blockIdx.x, xcd = bid & 7, rr = bid >> 3, br = xcd & 1, g = xcd >> 1, nt = rr & 7, p = rr >> 3;
  const unsigned rot = 2 * ((nt + 2 * p) & 7);
  const unsigned h = lane >> 5, l4 = lane >> 4, l15 = lane & 15;
  STAMP(ws, 1, 0);
  if (wid == 7) {
    const rsrc_t srdW = mkrsrc((const char*)(br ? woB : woA) + (size_t)g * 1024 * 2048 + nt * 256, 1024 * 2048 - nt * 256);
    const unsigned voffW = l4 * 2048 + ((l15 ^ (l4 << 2)) << 4);
    auto issue_w = [&](unsigned s) {
      const unsigned so = ((s + rot) & 15) * 64 * 2048, m = lds0 + (s & 3) * 16384;
#pragma unroll
      for (unsigned pc = 0; pc < 16; ++pc) dma16<W_NT>(m + pc * 1024, voffW, srdW, so + pc * 8192);
    };
    issue_w(0); issue_w(1);
#pragma unroll 1
    for (unsigned s = 0; s < 14; ++s) { wait_bar<16>(); issue_w(s + 2); }
    wait_bar<16>(); wait_bar<0>();
    return;
  }
  const unsigned q4 = (lane >> 2) & 3, pp = lane & 3, blk = l4 & 1;
  const unsigned wbase = lds0 + q4 * 256 + (q4 << 6) + (2 * blk + (pp & 1)) * 16 + (pp >> 1) * 8 + h * 2048;
  const char* ob = ws + WS_O + ((size_t)(((br * 4 + p) * 32 + 8 * g) * 7 + wid) * 8) * 1024 + lane * 16;
  auto load_o = [&](bf16x8 (&f)[4], unsigned s) {
    const unsigned kk = (s + rot) & 15;
    const char* a = ob + (kk >> 1) * 7 * 8192 + (kk & 1) * 4096;
#pragma unroll
    for (int i = 0; i < 4; ++i) f[i] = *(const GAS bf16x8*)(a + i * 1024);
  };
  f32x16 acc[4] = {};
  bf16x8 fa[4], fb[4];
  load_o(fa, 0); load_o(fb, 1);
#pragma unroll 1
  for (unsigned s = 0; s < 16; s += 2) {
    const unsigned wa = wbase + (s & 2) * 16384;
    bar();
    wstep(acc, wa, fa[0], fa[1]); wstep(acc, wa + 8192, fa[2], fa[3]);
    load_o(fa, s + 2 < 16 ? s + 2 : 15);
    bar();
    wstep(acc, wa + 16384, fb[0], fb[1]); wstep(acc, wa + 16384 + 8192, fb[2], fb[3]);
    load_o(fb, s + 3 < 16 ? s + 3 : 15);
  }
  STAMP(ws, 1, 1);
  char* pp_ = ws + WS_P + ((size_t)((((br * 4 + p) * 8 + nt) * 4 + g) * 7 + wid) * 16) * 1024 + lane * 16;
#pragma unroll
  for (int c = 0; c < 4; ++c)
#pragma unroll
    for (int q = 0; q < 4; ++q) { f32x4 v = {acc[c][4 * q], acc[c][4 * q + 1], acc[c][4 * q + 2], acc[c][4 * q + 3]}; *(GAS f32x4*)(pp_ + (c * 4 + q) * 1024) = v; }
  STAMP(ws, 1, 2);
}

__global__ __launch_bounds__(256)
void k_reduce(const char* __restrict__ ws, u16* __restrict__ out) {
  const unsigned tid = blockIdx.x * 256 + threadIdx.x, lane = tid & 63;
  unsigned w = tid >> 6;
  const unsigned s1 = w & 1; w >>= 1;
  const unsigned c = w & 3; w >>= 2;
  const unsigned j = w % 7; w /= 7;
  const unsigned nt = w & 7, p = (w >> 3) & 3, br = w >> 5;
  const char* pb = ws + WS_P + ((size_t)((((br * 4 + p) * 8 + nt) * 4) * 7 + j) * 16 + c * 4 + s1 * 2) * 1024 + lane * 16;
  f32x4 a = {}, b = {};
#pragma unroll
  for (int g = 0; g < 4; ++g) { a += *(const GAS f32x4*)(pb + (size_t)g * 7 * 16384); b += *(const GAS f32x4*)(pb + (size_t)g * 7 * 16384 + 1024); }
  const unsigned r = lane & 31, h = lane >> 5;
  const unsigned row = j < 6 ? (2 * p + j / 3) * SQ + (j % 3) * 32 + r : (2 * p + (r >> 4)) * SQ + 96 + (r & 15);
  u32x4 v = {cvtpk(a[0], a[1]), cvtpk(a[2], a[3]), cvtpk(b[0], b[1]), cvtpk(b[2], b[3])};
  *(GAS u32x4*)(out + (size_t)row * 2048 + br * 1024 + nt * 128 + c * 32 + s1 * 16 + h * 8) = v;
}

extern "C" void kernel_launch(void* const* d_in, const int* in_sizes, int n_in, void* d_out, int out_size, void* d_ws, size_t ws_size, hipStream_t stream) {
  (void)hipFuncSetAttribute((const void*)k_attn, hipFuncAttributeMaxDynamicSharedMemorySize, 163840);
  (void)hipFuncSetAttribute((const void*)k_oproj, hipFuncAttributeMaxDynamicSharedMemorySize, 65536);
  k_attn<<<256, 512, 163840, stream>>>((const u16*)d_in[0], (const u16*)d_in[1], (const u16*)d_in[2], (const u16*)d_in[3],
                                       (const u16*)d_in[5], (const u16*)d_in[6], (const u16*)d_in[7], (const u16*)d_in[8], (char*)d_ws);
  k_oproj<<<256, 512, 65536, stream>>>((const u16*)d_in[4], (const u16*)d_in[9], (char*)d_ws);
  k_reduce<<<896, 256, 0, stream>>>((const char*)d_ws, (u16*)d_out);
}
```

```cpp
#include <hip/hip_runtime.h>
#include <stdint.h>
#include <type_traits>

typedef unsigned short u16;
typedef __bf16 bf16x8 __attribute__((ext_vector_type(8)));
typedef __bf16 bf16x4 __attribute__((ext_vector_type(4)));
typedef __bf16 bf16x2 __attribute__((ext_vector_type(2)));
typedef float f32x16 __attribute__((ext_vector_type(16)));
typedef float f32x4 __attribute__((ext_vector_type(4)));
typedef float f32x2 __attribute__((ext_vector_type(2)));
typedef unsigned u32x4 __attribute__((ext_vector_type(4)));
typedef unsigned u32x2 __attribute__((ext_vector_type(2)));
#define LAS __attribute__((address_space(3)))
#define GAS __attribute__((address_space(1)))
#define DI __device__ __forceinline__
#define SB() __builtin_amdgcn_sched_barrier(0)
#define IC(n) std::integral_constant<int, n>{}
typedef __amdgpu_buffer_rsrc_t rsrc_t;

constexpr int SQ = 112, DM = 1024, NH = 32, SK = 1024, DH = 128;
constexpr size_t WS_O = 0;
constexpr size_t WS_X = (size_t)16 << 20;
constexpr size_t WS_F = (size_t)48 << 20;
constexpr size_t WS_ST = (size_t)64 << 20;
constexpr float QSCALE = 0.08838834764831845f * 1.4426950408889634f;
constexpr float THR = 8.0f;

#define DPOS 2
#define LA 2
#define CSTAMP(ws, kern, idx) do {} while (0)
#define STAMP(ws, kern, idx) do {} while (0)

DI rsrc_t mkrsrc(const void* p, unsigned bytes) { return __builtin_amdgcn_make_buffer_rsrc((void*)p, (short)0, (int)bytes, 0x00020000); }
template <int NT> DI void dma16(unsigned m0v, unsigned voff, rsrc_t srd, unsigned soff) {
  if constexpr (NT) asm volatile("s_mov_b32 m0, %0\n\ts_nop 0\n\tbuffer_load_dwordx4 %1, %2, %3 offen nt lds" ::"s"(m0v), "v"(voff), "s"(srd), "s"(soff) : "memory");
  else              asm volatile("s_mov_b32 m0, %0\n\ts_nop 0\n\tbuffer_load_dwordx4 %1, %2, %3 offen lds" ::"s"(m0v), "v"(voff), "s"(srd), "s"(soff) : "memory");
}
template <int N> DI void wait_bar() { asm volatile("s_waitcnt vmcnt(%0) lgkmcnt(0)\n\ts_barrier" ::"n"(N) : "memory"); }
DI void bar() { asm volatile("s_waitcnt lgkmcnt(0)\n\ts_barrier" ::: "memory"); }
DI void rawbar() { asm volatile("s_barrier" ::: "memory"); }
DI bf16x8 lds128(unsigned a) { return *(const LAS bf16x8*)(size_t)a; }
DI bf16x4 ldstr(unsigned a) { return __builtin_amdgcn_ds_read_tr16_b64_v4bf16((LAS bf16x4*)(size_t)a); }
DI bf16x8 cat(bf16x4 a, bf16x4 b) { return __builtin_shufflevector(a, b, 0, 1, 2, 3, 4, 5, 6, 7); }
DI f32x16 mfma32(bf16x8 a, bf16x8 b, f32x16 c) { return __builtin_amdgcn_mfma_f32_32x32x16_bf16(a, b, c, 0, 0, 0); }
DI f32x4 mfma16(bf16x8 a, bf16x8 b, f32x4 c) { return __builtin_amdgcn_mfma_f32_16x16x32_bf16(a, b, c, 0, 0, 0); }
DI unsigned cvtpk(float lo, float hi) { f32x2 v = {lo, hi}; return __builtin_bit_cast(unsigned, __builtin_convertvector(v, bf16x2)); }
DI bf16x8 pack8(const f32x16& a, int o, float s) {
  u32x4 w = {cvtpk(a[o] * s, a[o + 1] * s), cvtpk(a[o + 2] * s, a[o + 3] * s), cvtpk(a[o + 4] * s, a[o + 5] * s), cvtpk(a[o + 6] * s, a[o + 7] * s)};
  return __builtin_bit_cast(bf16x8, w);
}
DI bf16x8 pack8n(const f32x16& a, int o) {
  u32x4 w = {cvtpk(a[o], a[o + 1]), cvtpk(a[o + 2], a[o + 3]), cvtpk(a[o + 4], a[o + 5]), cvtpk(a[o + 6], a[o + 7])};
  return __builtin_bit_cast(bf16x8, w);
}
DI float fadd(float a, float b) { float r; asm("v_add_f32_e32 %0, %1, %2" : "=v"(r) : "v"(a), "v"(b)); return r; }
DI float max2f(float a, float b) { return __builtin_elementwise_maximum(a, b); }
DI float max3f(float a, float b, float c) { return max2f(max2f(a, b), c); }
DI float xhalf_max(float x) { auto r = __builtin_amdgcn_permlane32_swap(__float_as_uint(x), __float_as_uint(x), false, false); return max2f(__uint_as_float(r[0]), __uint_as_float(r[1])); }
DI float xhalf_sum(float x) { auto r = __builtin_amdgcn_permlane32_swap(__float_as_uint(x), __float_as_uint(x), false, false); return __uint_as_float(r[0]) + __uint_as_float(r[1]); }

constexpr unsigned QS1 = 0, QS2 = 73728, QS0 = 118784, QW = 28672;
constexpr unsigned LDS_Q = 98304;
struct Frag { bf16x8 a[4], b[7]; };

template <bool ISQ, class Tail>
DI void gemm_compute(f32x4 (&acc)[7][4], unsigned lds0, unsigned wid, Tail tail) {
  const unsigned lane = threadIdx.x & 63, l4 = lane >> 4, l15 = lane & 15, q4 = (lane >> 2) & 3, pp = lane & 3;
  const unsigned it = wid >> 1, dh = wid & 1;
  const unsigned abase = lds0 + QW + (8 * l4 + q4) * 256 + (((4 * dh) ^ ((q4 << 1) | (l4 & 1))) << 5) + pp * 8;
  const unsigned bbase = ISQ ? lds0 + it * 14336 + l15 * 128 + ((l4 ^ (l15 >> 1)) << 4) : lds0 + it * 7168 + lane * 16;
  Frag f0, f1;
  auto rd = [&](Frag& f, int i, unsigned slot, int s) {
    if (i < 4) { const unsigned a = (abase + slot) ^ (i << 5); f.a[i] = cat(ldstr(a + s * 8192), ldstr(a + s * 8192 + 1024)); }
    else if (i < 11) { const int j = i - 4; f.b[j] = ISQ ? lds128(((bbase + slot) ^ (s << 6)) + j * 2048) : lds128(bbase + slot + s * 14336 + j * 1024); }
  };
  auto mm = [&](Frag& f, int j) {
#pragma unroll
    for (int n = 0; n < 4; ++n) acc[j][n] = mfma16(f.a[n], f.b[j], acc[j][n]);
  };
  rawbar(); rawbar();
#pragma unroll
  for (int i = 0; i < 11; ++i) rd(f0, i, QS0, 0);
  auto step = [&](auto MODE, unsigned cur, unsigned nxt) {
    constexpr int M = decltype(MODE)::value;
    SB();
#pragma unroll
    for (int j = 0; j < 7; ++j) { mm(f0, j); rd(f1, 2 * j, cur, 1); rd(f1, 2 * j + 1, cur, 1); SB(); }
    bar();
    SB();
#pragma unroll
    for (int j = 0; j < 7; ++j) {
      mm(f1, j);
      if constexpr (M < 3) { rd(f0, 2 * j, nxt, 0); rd(f0, 2 * j + 1, nxt, 0); }
      if constexpr (M == 1 || M == 2) { if (j >= (M == 1 ? 1 : 3)) tail(M - 1, j - 3); }
      SB();
    }
  };
  unsigned cur = QS0, nxt = QS1, nx2 = QS2;
#pragma unroll 1
  for (unsigned v = 0; v < 13; ++v) { step(IC(0), cur, nxt); const unsigned t = cur; cur = nxt; nxt = nx2; nx2 = t; }
  step(IC(1), QS1, QS2);
  step(IC(2), QS2, QS0);
  step(IC(3), QS0, QS0);
}

template <bool ISQ, class Tail>
DI void gemm_load(const rsrc_t srdB, const rsrc_t srdW, unsigned lds0, unsigned lw, unsigned rot, Tail tail) {
  constexpr unsigned WROW = ISQ ? 8192 : 2048;
  const unsigned lane = threadIdx.x & 63, l4 = lane >> 4, l15 = lane & 15;
  const unsigned voffW = l4 * WROW + ((l15 ^ ((l4 << 2) | (lw & 2))) << 4);
  const unsigned vB0 = ISQ ? (lane >> 3) * 2048 + ((((lane & 7) ^ l4) << 4) ^ ((lw & 1) << 6)) : lane * 16, vB1 = ISQ ? vB0 ^ 64 : vB0;
  auto issue = [&](unsigned v, unsigned slot) {
    const unsigned kk = (v & 8) | ((v + rot) & 7);
    const unsigned m = lds0 + slot + lw * 7168, mw = lds0 + slot + QW + lw * 1024;
    const unsigned sb = ISQ ? lw * (56 * 2048) + kk * 128 : kk * 28672 + lw * 7168, sw = (kk * 64 + 4 * lw) * WROW;
#pragma unroll
    for (unsigned i = 0; i < 4; ++i) dma16<0>(mw + i * 4096, voffW, srdW, sw + i * 16 * WROW);
#pragma unroll
    for (unsigned i = 0; i < 7; ++i) dma16<0>(m + i * 1024, (i & 1) ? vB1 : vB0, srdB, sb + i * (ISQ ? 8 * 2048 : 1024));
  };
  issue(0, QS0); rawbar();
  issue(1, QS1);
  wait_bar<11>();
  issue(2, QS2);
  unsigned cur = QS0, nxt = QS1, nx2 = QS2;
#pragma unroll 1
  for (unsigned v = 0; v < 13; ++v) { wait_bar<11>(); issue(v + 3, cur); const unsigned t = cur; cur = nxt; nxt = nx2; nx2 = t; }
  wait_bar<11>();
#pragma unroll
  for (int i = -2; i < 4; ++i) tail(0, i);
  if constexpr (ISQ) wait_bar<6>(); else wait_bar<0>();
#pragma unroll
  for (int i = 0; i < 4; ++i) tail(1, i);
  rawbar();
}

__global__ __launch_bounds__(512, 2)
void k_attn(const u16* __restrict__ xA, const u16* __restrict__ kA, const u16* __restrict__ vA, const u16* __restrict__ wqA,
            const u16* __restrict__ xB, const u16* __restrict__ kB, const u16* __restrict__ vB, const u16* __restrict__ wqB, char* __restrict__ ws) {
  extern __shared__ __attribute__((aligned(16))) char smem[];
  const unsigned lds0 = (unsigned)(size_t)(LAS char*)smem;
  const unsigned lane = threadIdx.x & 63, wid = __builtin_amdgcn_readfirstlane(threadIdx.x >> 6);
  const unsigned bid = blockIdx.x, xcd = bid & 7, rr = bid >> 3, br = xcd & 1, g = xcd >> 1, hh = rr & 7, p = rr >> 3;
  const unsigned head = 8 * g + hh, it = wid >> 2, w4 = wid & 3, batch = 2 * p + it;
  STAMP(ws, 0, 0); CSTAMP(ws, 0, 4);
  const unsigned kvitem = batch * NH + head;
  const rsrc_t srdX = mkrsrc((const char*)(br ? xB : xA) + (size_t)p * 2 * SQ * DM * 2, 2 * SQ * DM * 2);
  const rsrc_t srdW = mkrsrc((const char*)(br ? wqB : wqA) + head * 256, DM * NH * DH * 2 - head * 256);
  const char* const kptr = (const char*)(br ? kB : kA) + (size_t)kvitem * SK * DH * 2;
  const char* const vptr = (const char*)(br ? vB : vA) + (size_t)kvitem * SK * DH * 2;
  char* const obase = ws + WS_O + (size_t)((br * 4 + p) * 32 + head) * 57344;
  asm volatile("" :: "s"(srdX), "s"(srdW), "s"(kptr), "s"(vptr), "s"(obase));
  const unsigned r32 = lane & 31, h = lane >> 5, l4 = lane >> 4, l15 = lane & 15;
  const unsigned q4 = (lane >> 2) & 3, pp = lane & 3;
  const unsigned voffV = l4 * 256 + ((l15 ^ (l4 << 2)) << 4);
  const unsigned voffK = l4 * 256 + ((l15 ^ ((w4 << 2) | l4)) << 4);
  const bool full = w4 < 3;
  constexpr int KVNT = 1;
  auto issue_kv1 = [&](int t, unsigned slot, unsigned i) {
    const unsigned m = lds0 + slot + it * 16384 + w4 * 1024 + i * 4096, so = t * 8192 + w4 * 1024 + (i & 1) * 4096;
    if (i < 2) dma16<KVNT>(m, voffK, mkrsrc(kptr, t < 31 ? SK * DH * 2 : 0), so + 8192);
    else dma16<KVNT>(m, voffV, mkrsrc(vptr, t < 32 ? SK * DH * 2 : 0), so);
  };
  auto tail = [&](unsigned t, int i) { if (i >= 0) issue_kv1(t, t * 65536, i); else issue_kv1(-1, 32768, i + 2); };

  if (wid < 4) {
    f32x4 acc[7][4] = {};
    gemm_compute<true>(acc, lds0, wid, tail);
    const unsigned qw = lds0 + LDS_Q + ((wid >> 1) * 112 + l15) * 256 + (l4 & 1) * 8;
#pragma unroll
    for (int n = 0; n < 4; ++n) {
      const unsigned a = qw + (((8 * (wid & 1) + 2 * n + (l4 >> 1)) ^ l15) << 4);
#pragma unroll
      for (int j = 0; j < 7; ++j) {
        u32x2 w = {cvtpk(acc[j][n][0] * QSCALE, acc[j][n][1] * QSCALE), cvtpk(acc[j][n][2] * QSCALE, acc[j][n][3] * QSCALE)};
        *(LAS u32x2*)(size_t)(a + j * 4096) = w;
      }
    }
  } else {
    if (wid == 4 && lane < 4) *(GAS unsigned*)(ws + WS_F + bid * 256 + lane * 64) = 0u;
    gemm_load<true>(srdX, srdW, lds0, w4, hh + 2 * p, tail);
  }
  wait_bar<8>();
  bf16x8 qf[8];
  {
    const unsigned qr = lds0 + LDS_Q + (it * 112 + 32 * w4 + (full ? r32 : l15)) * 256 + (((h ^ r32) & 15) << 4);
#pragma unroll
    for (int i = 0; i < 8; ++i) qf[i] = lds128(qr ^ (i << 5));
  }
  STAMP(ws, 0, 1); CSTAMP(ws, 0, 5);

  f32x16 o[4] = {};
  f32x16 negm = {};
  float mref = 0.f, lsum = 0.f;
  const unsigned trl = q4 * 256 + (q4 << 6) + (2 * (l4 & 1) + (pp & 1)) * 16 + (pp >> 1) * 8;
  const unsigned kbase = lds0 + it * 16384 + r32 * 256 + (((h ^ r32) & 15) << 4);
  const unsigned vbase = lds0 + it * 16384 + 8192 + trl + h * 1024;
  bf16x8 kf[8], vf[8];
  f32x16 s;
  auto rd_k = [&](int i, unsigned ka) { kf[i] = lds128(ka ^ (i << 5)); };
  auto qk = [&](int i) {
    if (i == 0) asm("v_mfma_f32_32x32x16_bf16 %0, %1, %2, %3" : "=&v"(s) : "v"(kf[0]), "v"(qf[0]), "v"(negm));
    else s = mfma32(kf[i], qf[i], s);
  };
  auto rowmax = [&](unsigned t) {
    float mx0 = max3f(s[0], s[1], s[2]), mx1 = max3f(s[3], s[4], s[5]);
    mx0 = max3f(mx0, s[6], s[7]); mx1 = max3f(mx1, s[8], s[9]);
    mx0 = max3f(mx0, s[10], s[11]); mx1 = max3f(mx1, s[12], s[13]);
    mx0 = max3f(mx0, s[14], s[15]);
    const float mx = xhalf_max(max2f(mx0, mx1));
    const float ninf = -__builtin_inff(), thr = t ? THR : ninf, lo = t ? 0.f : ninf;
    if (__builtin_expect(__any(mx > thr), 0)) {
      const float d = max2f(mx, lo), f = __builtin_amdgcn_exp2f(-max2f(d, 0.f));
      mref += d; lsum *= f;
#pragma unroll
      for (int i = 0; i < 16; ++i) { s[i] -= d; negm[i] = -mref; }
#pragma unroll
      for (int c = 0; c < 4; ++c)
#pragma unroll
        for (int i = 0; i < 16; ++i) o[c][i] *= f;
    }
  };
  auto pv = [&](auto pre, auto hook) {
#pragma unroll
    for (int i = 0; i < 4; ++i) { s[2 * i] = __builtin_amdgcn_exp2f(s[2 * i]); s[2 * i + 1] = __builtin_amdgcn_exp2f(s[2 * i + 1]); pre(i); SB(); }
    const bf16x8 p0 = pack8n(s, 0);
    SB();
#pragma unroll
    for (int c = 0; c < 4; ++c) { o[c] = mfma32(vf[c], p0, o[c]); s[8 + 2 * c] = __builtin_amdgcn_exp2f(s[8 + 2 * c]); s[9 + 2 * c] = __builtin_amdgcn_exp2f(s[9 + 2 * c]); hook(c); SB(); }
    const bf16x8 p1 = pack8n(s, 8);
    SB();
    float l0 = lsum, l1 = 0.f;
#pragma unroll
    for (int c = 0; c < 4; ++c) {
      o[c] = mfma32(vf[4 + c], p1, o[c]);
      l0 = fadd(fadd(l0, s[4 * c]), s[4 * c + 1]); l1 = c ? fadd(fadd(l1, s[4 * c + 2]), s[4 * c + 3]) : fadd(s[2], s[3]);
      hook(4 + c);
      SB();
    }
    lsum = fadd(l0, l1);
  };
  auto rd_v0 = [&](int c, unsigned va) { const unsigned a = va ^ (c << 6); vf[c] = cat(ldstr(a), ldstr(a + 2048)); };
  auto rd_v1 = [&](int c, unsigned va) { const unsigned a = va ^ (c << 6); vf[4 + c] = cat(ldstr(a + 4096), ldstr(a + 6144)); };
#pragma unroll
  for (int i = 0; i < 8; ++i) { rd_k(i, kbase + 32768); SB(); }
  unsigned slot = 0, slot1 = 65536, slotn = 32768;
#define ROTSLOT() do { const unsigned tmp = slot; slot = slot1; slot1 = slotn; slotn = tmp; } while (0)
  if (it == 0) {
#pragma unroll 1
    for (unsigned t = 0; t < 32; ++t) {
      wait_bar<4 * (LA - 1)>();
      const unsigned ka = kbase + slot, va = vbase + slot;
#pragma unroll
      for (int i = 0; i < 8; ++i) { qk(i); if (i >= 4) { rd_v0(i - 4, va); } SB(); }
      SB();
      rowmax(t);
      pv([&](int i) { rd_v1(i, va); }, [&](int c) { rd_k(c, ka); });
      for (int i = 0; i < 4; ++i) issue_kv1(t + LA, slotn, i);
      ROTSLOT();
    }
  } else {
    __builtin_amdgcn_s_setprio(1);
#pragma unroll
    for (int i = 0; i < 8; ++i) { qk(i); SB(); }
#pragma unroll 1
    for (unsigned t = 0; t < 31; ++t) {
      wait_bar<4 * (LA - 1)>();
      const unsigned ka = kbase + slot, va = vbase + slot;
      rd_v0(0, va); rd_v0(1, va); rd_v0(2, va); rd_v0(3, va);
      SB();
      rowmax(t);
      pv([&](int i) { rd_v1(i, va); }, [&](int c) { rd_k(c, ka); });
#pragma unroll
      for (int i = 0; i < 8; ++i) { qk(i); SB(); }
      for (int i = 0; i < 4; ++i) issue_kv1(t + LA, slotn, i);
      ROTSLOT();
    }
    wait_bar<4 * (LA - 1)>();
    const unsigned va = vbase + slot;
    rd_v0(0, va); rd_v0(1, va); rd_v0(2, va); rd_v0(3, va);
    SB();
    rowmax(31);
    pv([&](int i) { rd_v1(i, va); }, [&](int) {});
    __builtin_amdgcn_s_setprio(0);
  }
  STAMP(ws, 0, 2); CSTAMP(ws, 0, 6);
  const float inv = 1.f / xhalf_sum(lsum);
  char* op = obase + (it * 7 + 2 * w4 + (r32 >> 4)) * 1024 + (h * 16 + l15) * 16;
  if (full || r32 < 16) {
#pragma unroll
    for (int c = 0; c < 4; ++c) {
      *(GAS bf16x8*)(op + c * 14336) = pack8(o[c], 0, inv);
      *(GAS bf16x8*)(op + c * 14336 + 512) = pack8(o[c], 8, inv);
    }
  }
  STAMP(ws, 0, 3);
}

__global__ __launch_bounds__(512, 2)
void k_oproj(const u16* __restrict__ woA, const u16* __restrict__ woB, char* __restrict__ ws, u16* __restrict__ out) {
  extern __shared__ __attribute__((aligned(16))) char smem[];
  const unsigned lds0 = (unsigned)(size_t)(LAS char*)smem;
  const unsigned lane = threadIdx.x & 63, wid = __builtin_amdgcn_readfirstlane(threadIdx.x >> 6);
  const unsigned bid = blockIdx.x, xcd = bid & 7, rr = bid >> 3, br = xcd & 1, g = xcd >> 1, nt = rr & 7, p = rr >> 3;
  STAMP(ws, 1, 0);
  const rsrc_t srdO = mkrsrc(ws + WS_O + (size_t)((br * 4 + p) * 32 + 8 * g) * 57344, 8 * 57344);
  const rsrc_t srdW = mkrsrc((const char*)(br ? woB : woA) + (size_t)g * 1024 * 2048 + nt * 256, 1024 * 2048 - nt * 256);
  const rsrc_t srdE = mkrsrc(ws + WS_X, 256 * 98304);
  GAS unsigned* const flags = (GAS unsigned*)(ws + WS_F);
  u16* const outp = out + (size_t)(2 * p + (wid >> 1)) * SQ * 2048 + br * 1024 + nt * 128 + (wid & 1) * 64;
  asm volatile("" :: "s"(srdO), "s"(srdW), "s"(srdE), "s"(flags), "s"(outp));
  auto notail = [](unsigned, int) {};
  if (wid >= 4) { gemm_load<false>(srdO, srdW, lds0, wid - 4, nt + 2 * p, notail); return; }
  f32x4 acc[7][4] = {};
  gemm_compute<false>(acc, lds0, wid, notail);
  STAMP(ws, 1, 1);
  const unsigned bid0 = bid & ~6u;
#pragma unroll
  for (unsigned d = 0; d < 4; ++d)
    if (d != g) {
      const unsigned so = (bid0 + 2 * d) * 98304 + ((g - d - 1) & 3) * 32768 + wid * 8192;
#pragma unroll
      for (int jj = 0; jj < 2; ++jj)
#pragma unroll
        for (int n = 0; n < 4; ++n)
          if (2 * d + jj < 7) __builtin_amdgcn_raw_buffer_store_b128(__builtin_bit_cast(u32x4, acc[2 * d + jj][n]), srdE, lane * 16 + (jj * 4 + n) * 1024, so, 16);
    }
  f32x4 r[2][4];
#pragma unroll
  for (int n = 0; n < 4; ++n) {
    r[0][n] = g == 0 ? acc[0][n] : g == 1 ? acc[2][n] : g == 2 ? acc[4][n] : acc[6][n];
    r[1][n] = g == 0 ? acc[1][n] : g == 1 ? acc[3][n] : acc[5][n];
  }
  asm volatile("s_waitcnt vmcnt(0)\n\ts_barrier" ::: "memory");
  if (wid == 0) {
    if (lane < 4 && lane != g) __hip_atomic_store(flags + (bid0 + 2 * lane) * 64 + g * 16, 1u, __ATOMIC_RELAXED, __HIP_MEMORY_SCOPE_AGENT);
    for (unsigned spin = 0; spin < (1u << 20); ++spin) {
      const unsigned f = (lane < 4 && lane != g) ? __hip_atomic_load(flags + bid * 64 + lane * 16, __ATOMIC_RELAXED, __HIP_MEMORY_SCOPE_AGENT) : 1u;
      if (__all(f == 1u)) break;
      __builtin_amdgcn_s_sleep(1);
    }
  }
  asm volatile("s_waitcnt vmcnt(0)\n\ts_barrier" ::: "memory");
  STAMP(ws, 1, 2);
  const int njj = g == 3 ? 1 : 2;
  u32x4 e[3][2][4];
#pragma unroll
  for (int jj = 0; jj < 2; ++jj)
    if (jj < njj) {
#pragma unroll
      for (int sl = 0; sl < 3; ++sl)
#pragma unroll
        for (int n = 0; n < 4; ++n) e[sl][jj][n] = __builtin_amdgcn_raw_buffer_load_b128(srdE, lane * 16 + (jj * 4 + n) * 1024, bid * 98304 + sl * 32768 + wid * 8192, 16);
    }
#pragma unroll
  for (int jj = 0; jj < 2; ++jj)
    if (jj < njj) {
      u16* o = outp + (size_t)((2 * g + jj) * 16 + (lane & 15)) * 2048 + (lane >> 4) * 4;
#pragma unroll
      for (int n = 0; n < 4; ++n) {
        const f32x4 v = (r[jj][n] + __builtin_bit_cast(f32x4, e[0][jj][n])) + (__builtin_bit_cast(f32x4, e[1][jj][n]) + __builtin_bit_cast(f32x4, e[2][jj][n]));
        u32x2 w = {cvtpk(v[0], v[1]), cvtpk(v[2], v[3])};
        *(GAS u32x2*)(o + n * 16) = w;
      }
    }
  STAMP(ws, 1, 3);
}

extern "C" void kernel_launch(void* const* d_in, const int* in_sizes, int n_in, void* d_out, int out_size, void* d_ws, size_t ws_size, hipStream_t stream) {
  (void)hipFuncSetAttribute((const void*)k_attn, hipFuncAttributeMaxDynamicSharedMemorySize, 163840);
  (void)hipFuncSetAttribute((const void*)k_oproj, hipFuncAttributeMaxDynamicSharedMemorySize, 163840);
  k_attn<<<256, 512, 163840, stream>>>((const u16*)d_in[0], (const u16*)d_in[1], (const u16*)d_in[2], (const u16*)d_in[3],
                                       (const u16*)d_in[5], (const u16*)d_in[6], (const u16*)d_in[7], (const u16*)d_in[8], (char*)d_ws);
  k_oproj<<<256, 512, 163840, stream>>>((const u16*)d_in[4], (const u16*)d_in[9], (char*)d_ws, (u16*)d_out);
}
```

```cpp
#include <hip/hip_runtime.h>
#include <stdint.h>
#include <type_traits>

typedef unsigned short u16;
typedef unsigned long long u64;
typedef __bf16 bf16x8 __attribute__((ext_vector_type(8)));
typedef __bf16 bf16x4 __attribute__((ext_vector_type(4)));
typedef __bf16 bf16x2 __attribute__((ext_vector_type(2)));
typedef float f32x16 __attribute__((ext_vector_type(16)));
typedef float f32x4 __attribute__((ext_vector_type(4)));
typedef float f32x2 __attribute__((ext_vector_type(2)));
typedef unsigned u32x4 __attribute__((ext_vector_type(4)));
typedef unsigned u32x2 __attribute__((ext_vector_type(2)));
#define LAS __attribute__((address_space(3)))
#define GAS __attribute__((address_space(1)))
#define DI __device__ __forceinline__
#define SB() __builtin_amdgcn_sched_barrier(0)
#define LGKM(n) do { __builtin_amdgcn_s_waitcnt(0xC07F | ((n) << 8)); SB(); } while (0)
#define IC(n) std::integral_constant<int, n>{}
typedef __amdgpu_buffer_rsrc_t rsrc_t;

constexpr int SQ = 112, DM = 1024, NH = 32, SK = 1024, DH = 128;
constexpr size_t WS_O = 0;
constexpr size_t WS_X = (size_t)16 << 20;
constexpr size_t WS_F = (size_t)48 << 20;
constexpr size_t WS_G = WS_F + 65536;
constexpr size_t WS_E = WS_G + 524288;
constexpr size_t WS_ST = (size_t)64 << 20;
constexpr float QSCALE = 0.08838834764831845f * 1.4426950408889634f;
constexpr float THR = 8.0f;
constexpr unsigned SPIN = 1u << 18;

#define PMODE 1
#define PRIO_G(x)
#define PRIO_A(x) __builtin_amdgcn_s_setprio(x)
#define RMODE 3
#define RROT rot
#define RMSK 7
#define CSTAMP(kern, idx) do {} while (0)
#define STAMP(kern, idx) do {} while (0)

DI rsrc_t mkrsrc(const void* p, unsigned bytes) { return __builtin_amdgcn_make_buffer_rsrc((void*)p, (short)0, (int)bytes, 0x00020000); }
template <int POL> DI void dma16(unsigned m0v, unsigned voff, rsrc_t srd, unsigned soff) {
  if constexpr (POL == 1)      asm volatile("s_mov_b32 m0, %0\n\ts_nop 0\n\tbuffer_load_dwordx4 %1, %2, %3 offen nt lds" ::"s"(m0v), "v"(voff), "s"(srd), "s"(soff) : "memory");
  else if constexpr (POL == 2) asm volatile("s_mov_b32 m0, %0\n\ts_nop 0\n\tbuffer_load_dwordx4 %1, %2, %3 offen sc1 lds" ::"s"(m0v), "v"(voff), "s"(srd), "s"(soff) : "memory");
  else                         asm volatile("s_mov_b32 m0, %0\n\ts_nop 0\n\tbuffer_load_dwordx4 %1, %2, %3 offen lds" ::"s"(m0v), "v"(voff), "s"(srd), "s"(soff) : "memory");
}
template <int N> DI void wait_bar() { asm volatile("s_waitcnt vmcnt(%0) lgkmcnt(0)\n\ts_barrier" ::"n"(N) : "memory"); }
template <int N> DI void wait_vm() { asm volatile("s_waitcnt vmcnt(%0)" ::"n"(N) : "memory"); }
DI void bar() { asm volatile("s_waitcnt lgkmcnt(0)\n\ts_barrier" ::: "memory"); }
DI void rawbar() { asm volatile("s_barrier" ::: "memory"); }
DI bf16x8 lds128(unsigned a) { return *(const LAS bf16x8*)(size_t)a; }
DI bf16x4 ldstr(unsigned a) { return __builtin_amdgcn_ds_read_tr16_b64_v4bf16((LAS bf16x4*)(size_t)a); }
DI bf16x8 cat(bf16x4 a, bf16x4 b) { return __builtin_shufflevector(a, b, 0, 1, 2, 3, 4, 5, 6, 7); }
DI f32x16 mfma32(bf16x8 a, bf16x8 b, f32x16 c) { return __builtin_amdgcn_mfma_f32_32x32x16_bf16(a, b, c, 0, 0, 0); }
DI f32x4 mfma16(bf16x8 a, bf16x8 b, f32x4 c) { return __builtin_amdgcn_mfma_f32_16x16x32_bf16(a, b, c, 0, 0, 0); }
DI unsigned cvtpk(float lo, float hi) { f32x2 v = {lo, hi}; return __builtin_bit_cast(unsigned, __builtin_convertvector(v, bf16x2)); }
DI u32x4 pack8(const f32x16& a, int o, float s) {
  u32x4 w = {cvtpk(a[o] * s, a[o + 1] * s), cvtpk(a[o + 2] * s, a[o + 3] * s), cvtpk(a[o + 4] * s, a[o + 5] * s), cvtpk(a[o + 6] * s, a[o + 7] * s)};
  return w;
}
DI float fadd(float a, float b) { float r; asm("v_add_f32_e32 %0, %1, %2" : "=v"(r) : "v"(a), "v"(b)); return r; }
DI float max2f(float a, float b) { return __builtin_elementwise_maximum(a, b); }
DI float max3f(float a, float b, float c) { return max2f(max2f(a, b), c); }
DI float xhalf_max(float x) { auto r = __builtin_amdgcn_permlane32_swap(__float_as_uint(x), __float_as_uint(x), false, false); return max2f(__uint_as_float(r[0]), __uint_as_float(r[1])); }
DI float xhalf_sum(float x) { auto r = __builtin_amdgcn_permlane32_swap(__float_as_uint(x), __float_as_uint(x), false, false); return __uint_as_float(r[0]) + __uint_as_float(r[1]); }
DI void st_flag(char* p, u64 v) { __hip_atomic_store((GAS u64*)p, v, __ATOMIC_RELAXED, __HIP_MEMORY_SCOPE_AGENT); }
DI u64 ld_flag(const char* p) { return __hip_atomic_load((GAS u64*)p, __ATOMIC_RELAXED, __HIP_MEMORY_SCOPE_AGENT); }
DI void poll(const char* p, bool on, u64 tag) {
  for (unsigned spin = 0; spin < SPIN; ++spin) {
    const u64 f = on ? ld_flag(p) : tag;
    if (__all(f == tag)) break;
    __builtin_amdgcn_s_sleep(1);
  }
  asm volatile("" ::: "memory");
}

constexpr unsigned KV0 = 0, KV1 = 32768, KV2 = 65536, G0 = 98304, G1 = 129024, GW = 14336;

struct Frag { bf16x8 a[2], b[7]; };
template <bool ISQ> struct Gemm {
  unsigned abase, bbase;
  DI Gemm(unsigned lds0, unsigned cw) {
    const unsigned lane = threadIdx.x & 63, l4 = lane >> 4, l15 = lane & 15, q4 = (lane >> 2) & 3, pp = lane & 3;
    abase = lds0 + GW + (8 * l4 + q4) * 256 + (((2 * cw) ^ ((q4 << 1) | (l4 & 1))) << 5) + pp * 8;
    bbase = ISQ ? lds0 + l15 * 128 + ((l4 ^ (l15 >> 1)) << 4) : lds0 + lane * 16;
  }
  DI void rd(Frag& f, int i, unsigned slot, int s) const {
    if (i < 2) { const unsigned a = (abase + slot) ^ (i << 5); f.a[i] = cat(ldstr(a + s * 8192), ldstr(a + s * 8192 + 1024)); }
    else if (i < 9) { const int j = i - 2; f.b[j] = ISQ ? lds128(((bbase + slot) ^ (s << 6)) + j * 2048) : lds128(bbase + slot + s * 7168 + j * 1024); }
  }
  static DI void mm(f32x4 (&acc)[7][2], const Frag& f, int j) { acc[j][0] = mfma16(f.a[0], f.b[j], acc[j][0]); acc[j][1] = mfma16(f.a[1], f.b[j], acc[j][1]); }
  template <class Hook> DI void step(f32x4 (&acc)[7][2], unsigned slot, Hook hook) const {
    Frag f0, f1;
#pragma unroll
    for (int i = 0; i < 9; ++i) { rd(f0, i, slot, 0); SB(); }
    hook(0); SB();
#pragma unroll
    for (int j = 0; j < 7; ++j) { mm(acc, f0, j); rd(f1, 2 * j, slot, 1); rd(f1, 2 * j + 1, slot, 1); if (j & 1) hook(1 + (j >> 1)); SB(); }
#pragma unroll
    for (int j = 0; j < 7; ++j) { mm(acc, f1, j); if (!(j & 1)) hook(4 + (j >> 1)); SB(); }
  }
  DI void step(f32x4 (&acc)[7][2], unsigned slot) const { step(acc, slot, [](int) {}); }
  DI void run(f32x4 (&acc)[7][2], unsigned s0, unsigned s1, unsigned s2) const {
    Frag f0, f1;
#pragma unroll
    for (int i = 0; i < 9; ++i) rd(f0, i, s0, 0);
    auto half = [&](auto LAST, unsigned cur, unsigned nxt) {
      SB();
#pragma unroll
      for (int j = 0; j < 7; ++j) { mm(acc, f0, j); rd(f1, 2 * j, cur, 1); rd(f1, 2 * j + 1, cur, 1); SB(); }
      bar();
      SB();
#pragma unroll
      for (int j = 0; j < 7; ++j) { mm(acc, f1, j); if constexpr (!decltype(LAST)::value) { rd(f0, 2 * j, nxt, 0); rd(f0, 2 * j + 1, nxt, 0); } SB(); }
    };
#pragma unroll 1
    for (unsigned v = 0; v < 15; ++v) { half(IC(0), s0, s1); const unsigned t = s0; s0 = s1; s1 = s2; s2 = t; }
    half(IC(1), s0, s0);
  }
};
template <bool ISQ> struct GemmLoad {
  static constexpr unsigned WROW = ISQ ? 8192 : 2048;
  rsrc_t srdB, srdW;
  unsigned voffW, vB0, lds0, lw, rot, msk, bofs;
  DI GemmLoad(rsrc_t b, rsrc_t w, unsigned lds0_, unsigned lw_, unsigned rot_, unsigned msk_, unsigned bofs_) : srdB(b), srdW(w), lds0(lds0_), lw(lw_), rot(rot_), msk(msk_), bofs(bofs_) {
    const unsigned lane = threadIdx.x & 63, l4 = lane >> 4, l15 = lane & 15;
    voffW = l4 * WROW + ((l15 ^ ((l4 << 2) | (lw & 2))) << 4);
    vB0 = ISQ ? (lane >> 3) * 2048 + ((((lane & 7) ^ l4) << 4) ^ ((lw & 1) << 6)) : lane * 16;
  }
  DI unsigned kk(unsigned v) const {
    return (v & ~msk) | ((v + rot) & msk);
  }
  DI void w(unsigned v, unsigned slot) const {
    const unsigned m = lds0 + slot + GW + lw * 1024, so = (kk(v) * 64 + 4 * lw) * WROW;
#pragma unroll
    for (unsigned i = 0; i < 4; ++i) dma16<0>(m + i * 4096, voffW, srdW, so + i * 16 * WROW);
  }
  DI void b(unsigned v, unsigned slot) const {
    const unsigned m = lds0 + slot + lw * 1024, so = bofs + (ISQ ? lw * 16384 + kk(v) * 128 : kk(v) * 14336 + lw * 1024);
#pragma unroll
    for (unsigned i = 0; i < 4; ++i)
      if (i < 3 || lw < 2) dma16<ISQ ? 0 : 2>(m + i * 4096, vB0, srdB, so + i * (ISQ ? 65536 : 4096));
  }
  DI void tile(unsigned v, unsigned slot) const { w(v, slot); b(v, slot); }
  DI void piece(unsigned v, unsigned slot, unsigned i) const {
    if (i < 4) dma16<0>(lds0 + slot + GW + lw * 1024 + i * 4096, voffW, srdW, (kk(v) * 64 + 4 * lw + 16 * i) * WROW);
    else if (i < 7 || lw < 2)
      dma16<ISQ ? 0 : 2>(lds0 + slot + lw * 1024 + (i - 4) * 4096, vB0, srdB, bofs + (ISQ ? lw * 16384 + kk(v) * 128 + (i - 4) * 65536 : kk(v) * 14336 + lw * 1024 + (i - 4) * 4096));
  }
  template <int NT, int NB, int NX> DI void wbar() const { if (lw < 2) wait_bar<NT * 8 + NB * 4 + NX>(); else wait_bar<NT * 7 + NB * 3 + NX>(); }
};

struct Attn {
  f32x16 o[4], negm, sa, sb;
  bf16x8 qf[8], kf[8], vf[8];
  float mref, lsum;
  unsigned ka[8], va[4];
  DI void rd_k(int i, int ofs) { kf[i] = lds128(ka[i] + ofs); }
  DI void rd_v(int i, int ofs) { const unsigned a = va[i & 3] + ofs + (i >> 2) * 4096; vf[i] = cat(ldstr(a), ldstr(a + 2048)); }
  DI void qk(f32x16& s, int i) {
    if (i == 0) asm("v_mfma_f32_32x32x16_bf16 %0, %1, %2, %3" : "=&v"(s) : "v"(kf[0]), "v"(qf[0]), "v"(negm));
    else s = mfma32(kf[i], qf[i], s);
    if (i == 7) asm volatile("" :: "v"(negm));
  }
  template <bool FIRST> DI void rescale(f32x16& s, float mh) {
    if (FIRST || __builtin_expect(__any(mh > THR), 0)) {
      const float mx = xhalf_max(mh), d = FIRST ? mx : max2f(mx, 0.f);
      mref += d;
#pragma unroll
      for (int i = 0; i < 16; ++i) { s[i] -= d; negm[i] = -mref; }
      if constexpr (!FIRST) {
        const float f = __builtin_amdgcn_exp2f(-d);
        lsum *= f;
#pragma unroll
        for (int c = 0; c < 4; ++c)
#pragma unroll
          for (int i = 0; i < 16; ++i) o[c][i] *= f;
      }
      asm volatile("s_nop 1" : "+v"(negm));
    }
  }
  DI void advance(int delta) {
#pragma unroll
    for (int i = 0; i < 8; ++i) ka[i] += delta;
#pragma unroll
    for (int i = 0; i < 4; ++i) va[i] += delta;
  }
  DI void init(unsigned lds0, unsigned w4, unsigned qimg, unsigned kslot, unsigned slot0) {
    const unsigned lane = threadIdx.x & 63, r32 = lane & 31, h = lane >> 5, l4 = lane >> 4, q4 = (lane >> 2) & 3, pp = lane & 3;
    const unsigned kb = r32 * 256 + (((h ^ r32) & 15) << 4);
    const unsigned vb = 16384 + q4 * 256 + (q4 << 6) + (2 * (l4 & 1) + (pp & 1)) * 16 + (pp >> 1) * 8 + h * 1024;
    const unsigned qr = lds0 + qimg + (32 * w4 + (w4 < 3 ? r32 : lane & 15)) * 256 + (((h ^ r32) & 15) << 4);
#pragma unroll
    for (int i = 0; i < 8; ++i) { ka[i] = lds0 + kslot + (kb ^ (i << 5)); qf[i] = lds128(qr ^ (i << 5)); rd_k(i, 0); SB(); }
#pragma unroll
    for (int c = 0; c < 4; ++c) { o[c] = f32x16{}; va[c] = lds0 + kslot + (vb ^ (c << 6)); }
    negm = f32x16{}; mref = 0.f; lsum = 0.f;
    SB();
    asm volatile("s_nop 1" : "+v"(negm));
#pragma unroll
    for (int i = 0; i < 8; ++i) { qk(sa, i); SB(); }
#pragma unroll
    for (int i = 0; i < 8; ++i) rd_k(i, 8192);
    float m0 = max3f(sa[0], sa[1], sa[2]);
#pragma unroll
    for (int i = 3; i < 15; i += 2) m0 = max3f(m0, sa[i], sa[i + 1]);
    rescale<true>(sa, max2f(m0, sa[15]));
    advance(slot0 - kslot);
    SB();
  }
  template <bool NEXT, bool RDK> DI void sub(f32x16& c, f32x16& n, int ofs) {
    u32x4 p0, p1;
    LGKM(0);
#pragma unroll
    for (int i = 0; i < 8; ++i) {
      if constexpr (NEXT) { qk(n, i); SB(); }
      if (i < 4) rd_v(i, ofs);
      c[2 * i] = __builtin_amdgcn_exp2f(c[2 * i]); c[2 * i + 1] = __builtin_amdgcn_exp2f(c[2 * i + 1]);
      if (i > 0) { const unsigned w = cvtpk(c[2 * i - 2], c[2 * i - 1]); if (i <= 4) p0[i - 1] = w; else p1[i - 5] = w; }
      SB();
    }
    p1[3] = cvtpk(c[14], c[15]);
    LGKM(0);
    SB();
    float m0, m1, l0 = lsum, l1;
#pragma unroll
    for (int i = 0; i < 8; ++i) {
      if (RDK && i == 4) LGKM(7);
      if (RDK && i == 6) LGKM(5);
      o[i & 3] = mfma32(vf[i], __builtin_bit_cast(bf16x8, i < 4 ? p0 : p1), o[i & 3]);
      SB();
      if (i < 4) rd_v(4 + i, ofs);
      if constexpr (RDK) { if (i < 4) rd_k(i, ofs); else if (i < 6) { rd_k(2 * i - 4, ofs); rd_k(2 * i - 3, ofs); } }
      if constexpr (NEXT) {
        if (i == 3) { m0 = max3f(n[0], n[1], n[2]); m1 = max3f(n[3], n[4], n[5]); }
        if (i == 4) { m0 = max3f(m0, n[6], n[7]); m1 = max3f(m1, n[8], n[9]); }
        if (i == 5) { m0 = max3f(m0, n[10], n[11]); m1 = max3f(m1, n[12], n[13]); }
        if (i == 6) { m0 = max3f(m0, n[14], n[15]); }
        if (i == 7) { m0 = max2f(m0, m1); }
      }
      if (i < 2) { l0 = fadd(l0, c[2 * i]); l1 = i ? fadd(l1, c[3]) : c[1]; }
      else if (i < 6) { if (i < 4) { l0 = fadd(l0, c[2 * i]); l1 = fadd(l1, c[2 * i + 1]); } }
      else { l0 = fadd(fadd(l0, c[4 * i - 16]), c[4 * i - 14]); l1 = fadd(fadd(l1, c[4 * i - 15]), c[4 * i - 13]); }
      SB();
    }
    lsum = fadd(l0, l1);
    if constexpr (NEXT) rescale<false>(n, m0);
  }
  DI void run(unsigned s0, unsigned s1, unsigned s2) {
    wait_bar<8>();
#pragma unroll 1
    for (unsigned n = 0; n < 15; ++n) {
      sub<true, true>(sa, sb, 0);
      rawbar();
      sub<true, true>(sb, sa, 8192);
      advance(s1 - s0);
      const unsigned t = s0; s0 = s1; s1 = s2; s2 = t;
      wait_bar<0>();
    }
    sub<true, false>(sa, sb, 0);
    rawbar();
    sub<false, false>(sb, sa, 8192);
  }
  DI void store(rsrc_t srdO, unsigned so, unsigned w4) {
    const unsigned lane = threadIdx.x & 63, r32 = lane & 31, h = lane >> 5;
    const float inv = 1.f / xhalf_sum(lsum);
    const unsigned vo = (w4 < 3 || r32 < 16) ? so + (2 * w4 + (r32 >> 4)) * 1024 + (h * 16 + (lane & 15)) * 16 : 0x80000000u;
#pragma unroll
    for (int c = 0; c < 4; ++c) {
      __builtin_amdgcn_raw_buffer_store_b128(pack8(o[c], 0, inv), srdO, vo + c * 7168, 0, 16);
      __builtin_amdgcn_raw_buffer_store_b128(pack8(o[c], 8, inv), srdO, vo + c * 7168 + 512, 0, 16);
    }
  }
};

DI void write_q(const f32x4 (&acc)[7][2], unsigned base, unsigned cw) {
  const unsigned lane = threadIdx.x & 63, l4 = lane >> 4, l15 = lane & 15;
#pragma unroll
  for (int n = 0; n < 2; ++n) {
    const unsigned a = base + l15 * 256 + (l4 & 1) * 8 + (((4 * cw + 2 * n + (l4 >> 1)) ^ l15) << 4);
#pragma unroll
    for (int j = 0; j < 7; ++j) {
      u32x2 w = {cvtpk(acc[j][n][0] * QSCALE, acc[j][n][1] * QSCALE), cvtpk(acc[j][n][2] * QSCALE, acc[j][n][3] * QSCALE)};
      *(LAS u32x2*)(size_t)(a + j * 4096) = w;
    }
  }
}

__global__ __launch_bounds__(512, 2)
void k_fused(const u16* __restrict__ xA, const u16* __restrict__ kA, const u16* __restrict__ vA, const u16* __restrict__ wqA, const u16* __restrict__ woA,
             const u16* __restrict__ xB, const u16* __restrict__ kB, const u16* __restrict__ vB, const u16* __restrict__ wqB, const u16* __restrict__ woB,
             char* __restrict__ ws, u16* __restrict__ out) {
  extern __shared__ __attribute__((aligned(16))) char smem[];
  const unsigned lds0 = (unsigned)(size_t)(LAS char*)smem;
  const unsigned lane = threadIdx.x & 63, wid = __builtin_amdgcn_readfirstlane(threadIdx.x >> 6), w4 = wid & 3;
  const unsigned bid = blockIdx.x, xcd = bid & 7, rr = bid >> 3, br = xcd & 1, g = xcd >> 1, hh = rr & 7, p = rr >> 3;
  const unsigned head = 8 * g + hh, rot = hh + 2 * p;
  STAMP(0, 0); CSTAMP(0, 4);
  const char* const kptr = (const char*)(br ? kB : kA) + (size_t)(2 * p * NH + head) * (SK * DH * 2);
  const char* const vptr = (const char*)(br ? vB : vA) + (size_t)(2 * p * NH + head) * (SK * DH * 2);
  const rsrc_t srdX = mkrsrc((const char*)(br ? xB : xA) + (size_t)p * (2 * SQ * DM * 2), 2 * SQ * DM * 2);
  const rsrc_t srdWq = mkrsrc((const char*)(br ? wqB : wqA) + head * 256, DM * NH * DH * 2 - head * 256);
  const rsrc_t srdWo = mkrsrc((const char*)(br ? woB : woA) + (size_t)g * (1024 * 2048) + hh * 256, 1024 * 2048 - hh * 256);
  const rsrc_t srdO = mkrsrc(ws + WS_O + (size_t)((br * 8 + 2 * p) * 32 + 8 * g) * 28672, (32 + 8) * 28672);
  const rsrc_t srdE = mkrsrc(ws + WS_X, 2 * 256 * 65536);
  char* const epoch = ws + WS_E + (br * 4 + p) * 64;
  char* const fam = ws + WS_F + ((br * 4 + g) * 8 + 2 * p) * 512;
  u16* const outp = out + (size_t)(2 * p * SQ) * 2048 + br * 1024 + hh * 128 + w4 * 32;
  const u64 tag = *(const GAS u64*)epoch + 1;
  asm volatile("" :: "s"(kptr), "s"(vptr), "s"(srdX), "s"(srdWq), "s"(srdWo), "s"(srdO), "s"(srdE), "s"(outp), "s"(tag));
  const unsigned l4 = lane >> 4, l15 = lane & 15;
  const unsigned voffV = l4 * 256 + ((l15 ^ (l4 << 2)) << 4), voffK = l4 * 256 + ((l15 ^ ((w4 << 2) | l4)) << 4);
  constexpr unsigned KVBYTES = NH * SK * DH * 2 + SK * DH * 2;
  auto issue_k = [&](int n, unsigned slot) {
    const rsrc_t r = mkrsrc(kptr, n < 31 ? KVBYTES : 0);
    const unsigned so = ((n + 1) >> 4) * (NH * SK * DH * 2) + ((n + 1) & 15) * 16384 + w4 * 1024;
#pragma unroll
    for (unsigned i = 0; i < 4; ++i) dma16<1>(lds0 + slot + w4 * 1024 + i * 4096, voffK, r, so + i * 4096);
  };
  auto issue_kv = [&](int n, unsigned slot) {
    issue_k(n, slot);
    const rsrc_t r = mkrsrc(vptr, n < 32 ? KVBYTES : 0);
    const unsigned so = (n >> 4) * (NH * SK * DH * 2) + (n & 15) * 16384 + w4 * 1024;
#pragma unroll
    for (unsigned i = 0; i < 4; ++i) dma16<1>(lds0 + slot + 16384 + w4 * 1024 + i * 4096, voffV, r, so + i * 4096);
  };
  auto piece_k = [&](int n, unsigned slot, unsigned i) {
    dma16<1>(lds0 + slot + w4 * 1024 + i * 4096, voffK, mkrsrc(kptr, n < 31 ? KVBYTES : 0),
             ((n + 1) >> 4) * (NH * SK * DH * 2) + ((n + 1) & 15) * 16384 + w4 * 1024 + i * 4096);
  };
  auto piece_v = [&](int n, unsigned slot, unsigned i) {
    dma16<1>(lds0 + slot + 16384 + w4 * 1024 + i * 4096, voffV, mkrsrc(vptr, n < 32 ? KVBYTES : 0),
             (n >> 4) * (NH * SK * DH * 2) + (n & 15) * 16384 + w4 * 1024 + i * 4096);
  };
  auto side_step = [&](const auto& gm, const auto& gl, f32x4 (&acc)[7][2], unsigned v, unsigned gc, unsigned gn, int n, unsigned ks) {
    PRIO_G(1);
    gm.step(acc, gc, [&](int i) { if (i & 1) gl.piece(v + 1, gn, 4 + (i >> 1)); else piece_k(n + 2, ks, i >> 1); });
    PRIO_G(0);
    bar();
#pragma unroll
    for (unsigned i = 0; i < 4; ++i) { gl.piece(v + 2, gc, i); piece_v(n + 2, ks, i); }
  };
  auto kv_step = [&](int n, unsigned ks) {
#pragma unroll
    for (unsigned i = 0; i < 4; ++i) piece_k(n + 2, ks, i);
    rawbar();
#pragma unroll
    for (unsigned i = 0; i < 4; ++i) piece_v(n + 2, ks, i);
  };
  const unsigned bid0 = bid & ~6u;
  auto send = [&](const f32x4 (&acc)[7][2], unsigned r, bool own) {
#pragma unroll
    for (unsigned d = 0; d < 4; ++d)
      if (own || d != g) {
        const unsigned so = (r * 256 + bid0 + 2 * d) * 65536 + g * 16384 + w4 * 4096;
#pragma unroll
        for (int jj = 0; jj < 2; ++jj)
#pragma unroll
          for (int n = 0; n < 2; ++n)
            if (2 * d + jj < 7) __builtin_amdgcn_raw_buffer_store_b128(__builtin_bit_cast(u32x4, acc[2 * d + jj][n]), srdE, so + lane * 16 + (jj * 2 + n) * 1024, 0, 16);
      }
  };
  auto signal = [&](unsigned r) { if (lane < 4) st_flag(ws + WS_G + (((r * 256 + bid0 + 2 * lane) * 4 + w4) * 4 + g) * 64, tag); };
  auto finish = [&](unsigned r, bool own, f32x4 (&t)[2][2]) {
    poll(ws + WS_G + (((r * 256 + bid) * 4 + w4) * 4 + (lane & 3)) * 64, lane < 4, tag);
    u32x4 e[4][2][2];
#pragma unroll
    for (unsigned sr = 0; sr < 4; ++sr) {
      const unsigned src = own ? sr : (g + sr) & 3;
      if (own || sr) {
#pragma unroll
        for (int jj = 0; jj < 2; ++jj)
#pragma unroll
          for (int n = 0; n < 2; ++n)
            e[sr][jj][n] = __builtin_amdgcn_raw_buffer_load_b128(srdE, (jj && g == 3) ? 0x80000000u : lane * 16 + (jj * 2 + n) * 1024, (r * 256 + bid) * 65536 + src * 16384 + w4 * 4096, 16);
      }
    }
#pragma unroll
    for (int jj = 0; jj < 2; ++jj) {
      u16* o = outp + (size_t)(r * SQ + (2 * g + jj) * 16 + l15) * 2048 + l4 * 4;
#pragma unroll
      for (int n = 0; n < 2; ++n) {
        const f32x4 v = ((own ? __builtin_bit_cast(f32x4, e[0][jj][n]) : t[jj][n]) + __builtin_bit_cast(f32x4, e[1][jj][n])) +
                        (__builtin_bit_cast(f32x4, e[2][jj][n]) + __builtin_bit_cast(f32x4, e[3][jj][n]));
        u32x2 w = {cvtpk(v[0], v[1]), cvtpk(v[2], v[3])};
        if (!(jj && g == 3)) *(GAS u32x2*)(o + n * 16) = w;
      }
    }
  };

  if (wid < 4) {
    Attn at;
    {
      f32x4 acc[7][2] = {};
      const Gemm<true> gm(lds0, w4);
      rawbar(); rawbar();
      gm.run(acc, G0, KV1, G1);
      write_q(acc, lds0 + G0, w4);
    }
    bar();
    STAMP(0, 1); CSTAMP(0, 5);
    PRIO_A(1);
    at.init(lds0, w4, G0, KV2, KV0);
    at.run(KV0, KV1, KV2);
    PRIO_A(0);
    STAMP(0, 2); CSTAMP(0, 6);
    const Gemm<false> gm(lds0, w4);
    const GemmLoad<false> gl(srdO, srdWo, lds0, w4, RROT, RMSK, 0);
    f32x4 acc[7][2] = {};
    bar();
    at.store(srdO, hh * 28672, w4);
    kv_step(16, KV0);
    wait_bar<8>();
    if (wid == 0 && lane == 0) st_flag(fam + hh * 64, tag);
    kv_step(17, KV1);
    wait_bar<8>();
    {
      u64 f = 0;
      if (wid == 0) { const char* a = fam + (lane & 7) * 64; asm volatile("global_load_dwordx2 %0, %1, off sc1" : "=v"(f) : "v"(a) : "memory"); }
      kv_step(18, KV2);
      asm volatile("s_waitcnt vmcnt(8)" : "+v"(f) :: "memory");
      if (wid == 0 && !__all(f == tag)) poll(fam + (lane & 7) * 64, true, tag);
    }
    wait_bar<8>();
#pragma unroll
    for (unsigned i = 0; i < 4; ++i) { gl.piece(0, G0, i); piece_k(21, KV0, i); gl.piece(0, G0, 4 + i); }
    rawbar();
#pragma unroll
    for (unsigned i = 0; i < 4; ++i) { gl.piece(1, G1, i); piece_v(21, KV0, i); }
    {
      unsigned ks = KV1, ks1 = KV2, ks2 = KV0, gc = G0, gn = G1;
#pragma unroll 1
      for (unsigned v = 0; v < 12; ++v) {
        wait_bar<8>();
        side_step(gm, gl, acc, v, gc, gn, 20 + v, ks);
        const unsigned t = ks; ks = ks1; ks1 = ks2; ks2 = t;
        const unsigned u = gc; gc = gn; gn = u;
      }
    }
    STAMP(1, 0);
    wait_bar<8>();
    gl.b(13, G1); gl.tile(14, KV1); gl.tile(15, KV2);
    gm.step(acc, G0);
    gl.template wbar<2, 0, 0>();
    gm.step(acc, G1);
    gl.template wbar<1, 0, 0>();
    gm.step(acc, KV1);
    wait_bar<0>();
    gm.step(acc, KV2);
    send(acc, 0, true);
    STAMP(1, 1);
    bar();
#pragma unroll
    for (int j = 0; j < 7; ++j) { acc[j][0] = f32x4{}; acc[j][1] = f32x4{}; }
    wait_bar<0>();
    STAMP(1, 2);
    signal(0);
    gm.run(acc, G0, G1, KV0);
    STAMP(1, 3);
    send(acc, 1, false);
    f32x4 t[2][2];
#pragma unroll
    for (int n = 0; n < 2; ++n) {
      t[0][n] = g == 0 ? acc[0][n] : g == 1 ? acc[2][n] : g == 2 ? acc[4][n] : acc[6][n];
      t[1][n] = g == 0 ? acc[1][n] : g == 1 ? acc[3][n] : acc[5][n];
    }
    wait_vm<0>();
    STAMP(1, 4);
    signal(1);
    finish(1, false, t);
    if (g == 0 && hh == 0 && wid == 0 && lane == 0) st_flag(epoch, tag);
    STAMP(1, 5); STAMP(1, 6); STAMP(1, 7); STAMP(0, 3);
  } else {
    Attn at;
    {
      const GemmLoad<true> gl(srdX, srdWq, lds0, w4, 2 * rot, 15, 0);
      gl.tile(0, G0); rawbar();
      gl.tile(1, KV1);
      gl.template wbar<1, 0, 0>();
      gl.tile(2, G1);
      unsigned s0 = G0, s1 = KV1, s2 = G1;
#pragma unroll 1
      for (unsigned v = 0; v < 13; ++v) { gl.template wbar<1, 0, 0>(); gl.tile(v + 3, s0); const unsigned t = s0; s0 = s1; s1 = s2; s2 = t; }
      gl.template wbar<1, 0, 0>();
      issue_k(-1, KV2); issue_kv(0, KV0);
      wait_bar<12>();
    }
    const GemmLoad<true> gl(srdX, srdWq, lds0, w4, RROT, RMSK, SQ * DM * 2);
    gl.tile(0, G1);
    issue_kv(1, KV1);
    rawbar();
    gl.template wbar<1, 0, 16>();
    STAMP(0, 1); CSTAMP(0, 5);
    {
      const Gemm<true> gm(lds0, w4);
      f32x4 acc[7][2] = {};
      unsigned ks = KV2, ks1 = KV0, ks2 = KV1, gc = G1, gn = G0;
      wait_bar<8>();
      gl.w(1, G0);
#pragma unroll 1
      for (unsigned n = 0; n < 14; ++n) {
        side_step(gm, gl, acc, n, gc, gn, n, ks);
        const unsigned t = ks; ks = ks1; ks1 = ks2; ks2 = t;
        const unsigned u = gc; gc = gn; gn = u;
        wait_bar<8>();
      }
      PRIO_G(1);
      gm.step(acc, G1, [&](int i) { if (i & 1) gl.piece(15, G0, 4 + (i >> 1)); else piece_k(16, KV1, i >> 1); });
      PRIO_G(0);
      bar();
#pragma unroll
      for (unsigned i = 0; i < 4; ++i) piece_v(16, KV1, i);
      wait_bar<4>();
      PRIO_G(1);
      gm.step(acc, G0, [&](int i) { if (!(i & 1)) piece_k(17, KV2, i >> 1); });
      write_q(acc, lds0 + G1, w4);
      PRIO_G(0);
    }
    bar();
#pragma unroll
    for (unsigned i = 0; i < 4; ++i) piece_v(17, KV2, i);
    at.init(lds0, w4, G1, KV0, KV1);
    STAMP(0, 2); CSTAMP(0, 6);
    PRIO_A(1);
    at.run(KV1, KV2, KV0);
    PRIO_A(0);
    STAMP(1, 0);
    bar();
    at.store(srdO, (32 + hh) * 28672, w4);
    rawbar();
    wait_bar<0>();
    if (wid == 4 && lane == 0) st_flag(fam + 512 + hh * 64, tag);
    const GemmLoad<false> go(srdO, srdWo, lds0, w4, 2 * rot, 15, 32 * 28672);
    go.w(0, G0); go.w(1, G1);
    rawbar();
    go.w(2, KV0);
    STAMP(1, 1);
    if (wid == 4) poll(fam + 512 + (lane & 7) * 64, true, tag);
    rawbar();
    STAMP(1, 2);
    go.b(0, G0); go.b(1, G1);
    go.template wbar<0, 1, 0>();
    go.b(2, KV0);
    go.template wbar<0, 1, 0>();
    go.tile(3, G0);
    unsigned s0 = G1, s1 = KV0, s2 = G0;
#pragma unroll 1
    for (unsigned v = 1; v < 13; ++v) { go.template wbar<1, 0, 0>(); go.tile(v + 3, s0); const unsigned t = s0; s0 = s1; s1 = s2; s2 = t; }
    go.template wbar<1, 0, 0>();
    wait_bar<0>();
    rawbar();
    STAMP(1, 3); STAMP(1, 4);
    f32x4 t[2][2] = {};
    finish(0, true, t);
    STAMP(1, 5); STAMP(1, 6); STAMP(1, 7); STAMP(0, 3);
  }
}

extern "C" void kernel_launch(void* const* d_in, const int* in_sizes, int n_in, void* d_out, int out_size, void* d_ws, size_t ws_size, hipStream_t stream) {
  (void)hipFuncSetAttribute((const void*)k_fused, hipFuncAttributeMaxDynamicSharedMemorySize, 163840);
  k_fused<<<256, 512, 163840, stream>>>((const u16*)d_in[0], (const u16*)d_in[1], (const u16*)d_in[2], (const u16*)d_in[3], (const u16*)d_in[4],
                                        (const u16*)d_in[5], (const u16*)d_in[6], (const u16*)d_in[7], (const u16*)d_in[8], (const u16*)d_in[9],
                                        (char*)d_ws, (u16*)d_out);
}
```

```cpp
#include <hip/hip_runtime.h>
#include <stdint.h>
#include <type_traits>

typedef unsigned short u16;
typedef unsigned long long u64;
typedef __bf16 bf16x8 __attribute__((ext_vector_type(8)));
typedef __bf16 bf16x4 __attribute__((ext_vector_type(4)));
typedef __bf16 bf16x2 __attribute__((ext_vector_type(2)));
typedef float f32x16 __attribute__((ext_vector_type(16)));
typedef float f32x4 __attribute__((ext_vector_type(4)));
typedef float f32x2 __attribute__((ext_vector_type(2)));
typedef unsigned u32x4 __attribute__((ext_vector_type(4)));
typedef unsigned u32x2 __attribute__((ext_vector_type(2)));
#define LAS __attribute__((address_space(3)))
#define GAS __attribute__((address_space(1)))
#define DI __device__ __forceinline__
#define SB() __builtin_amdgcn_sched_barrier(0)
#define LGKM(n) do { __builtin_amdgcn_s_waitcnt(0xC07F | ((n) << 8)); SB(); } while (0)
#define IC(n) std::integral_constant<int, n>{}
typedef __amdgpu_buffer_rsrc_t rsrc_t;

constexpr int SQ = 112, DM = 1024, NH = 32, SK = 1024, DH = 128;
constexpr size_t WS_O = 0;
constexpr size_t WS_X = (size_t)16 << 20;
constexpr size_t WS_F = (size_t)48 << 20;
constexpr size_t WS_G = WS_F + 131072;
constexpr size_t WS_E = WS_G + 524288;
constexpr size_t WS_ST = (size_t)64 << 20;
constexpr float QSCALE = 0.08838834764831845f * 1.4426950408889634f;
constexpr float THR = 8.0f;
constexpr unsigned SPIN = 1u << 18;

#define NRT 5
#define RTILES {2, 5, 8, 11, 14, 0}
#define RBURST {2, 4, 7, 9, 11, 99}
#define CSTAMP(kern, idx) do {} while (0)
#define STAMP(kern, idx) do {} while (0)

DI rsrc_t mkrsrc(const void* p, unsigned bytes) { return __builtin_amdgcn_make_buffer_rsrc((void*)p, (short)0, (int)bytes, 0x00020000); }
template <int POL> DI void dma16(unsigned mb, unsigned mi, unsigned voff, rsrc_t srd, unsigned sb, unsigned si) {
  unsigned t;
  if constexpr (POL == 1)      asm volatile("s_add_u32 m0, %1, %2\n\ts_add_u32 %0, %5, %6\n\tbuffer_load_dwordx4 %3, %4, %0 offen nt lds"  : "=&s"(t) : "s"(mb), "n"(mi), "v"(voff), "s"(srd), "s"(sb), "n"(si) : "memory", "scc");
  else if constexpr (POL == 2) asm volatile("s_add_u32 m0, %1, %2\n\ts_add_u32 %0, %5, %6\n\tbuffer_load_dwordx4 %3, %4, %0 offen sc1 lds" : "=&s"(t) : "s"(mb), "n"(mi), "v"(voff), "s"(srd), "s"(sb), "n"(si) : "memory", "scc");
  else                         asm volatile("s_add_u32 m0, %1, %2\n\ts_add_u32 %0, %5, %6\n\tbuffer_load_dwordx4 %3, %4, %0 offen lds"     : "=&s"(t) : "s"(mb), "n"(mi), "v"(voff), "s"(srd), "s"(sb), "n"(si) : "memory", "scc");
}
DI void ld16nt(u32x4& d, unsigned voff, rsrc_t srd, unsigned sb, unsigned si) {
  unsigned t;
  asm volatile("s_add_u32 %1, %4, %5\n\tbuffer_load_dwordx4 %0, %2, %3, %1 offen nt" : "=v"(d), "=&s"(t) : "v"(voff), "s"(srd), "s"(sb), "n"(si) : "memory", "scc");
}
template <int N> DI void wait_bar() { asm volatile("s_waitcnt vmcnt(%0) lgkmcnt(0)\n\ts_barrier" ::"n"(N) : "memory"); }
template <int N> DI void wait_vm() { asm volatile("s_waitcnt vmcnt(%0)" ::"n"(N) : "memory"); }
DI void bar() { asm volatile("s_waitcnt lgkmcnt(0)\n\ts_barrier" ::: "memory"); }
DI void rawbar() { asm volatile("s_barrier" ::: "memory"); }
DI bf16x8 lds128(unsigned a) { return *(const LAS bf16x8*)(size_t)a; }
DI bf16x4 ldstr(unsigned a) { return __builtin_amdgcn_ds_read_tr16_b64_v4bf16((LAS bf16x4*)(size_t)a); }
DI bf16x8 cat(bf16x4 a, bf16x4 b) { return __builtin_shufflevector(a, b, 0, 1, 2, 3, 4, 5, 6, 7); }
DI f32x16 mfma32(bf16x8 a, bf16x8 b, f32x16 c) { return __builtin_amdgcn_mfma_f32_32x32x16_bf16(a, b, c, 0, 0, 0); }
DI f32x4 mfma16(bf16x8 a, bf16x8 b, f32x4 c) { return __builtin_amdgcn_mfma_f32_16x16x32_bf16(a, b, c, 0, 0, 0); }
DI unsigned cvtpk(float lo, float hi) { f32x2 v = {lo, hi}; return __builtin_bit_cast(unsigned, __builtin_convertvector(v, bf16x2)); }
DI u32x4 pack8(const f32x16& a, int o, float s) {
  u32x4 w = {cvtpk(a[o] * s, a[o + 1] * s), cvtpk(a[o + 2] * s, a[o + 3] * s), cvtpk(a[o + 4] * s, a[o + 5] * s), cvtpk(a[o + 6] * s, a[o + 7] * s)};
  return w;
}
DI float fadd(float a, float b) { float r; asm("v_add_f32_e32 %0, %1, %2" : "=v"(r) : "v"(a), "v"(b)); return r; }
DI float max2f(float a, float b) { return __builtin_elementwise_maximum(a, b); }
DI float max3f(float a, float b, float c) { return max2f(max2f(a, b), c); }
DI float xhalf_max(float x) { auto r = __builtin_amdgcn_permlane32_swap(__float_as_uint(x), __float_as_uint(x), false, false); return max2f(__uint_as_float(r[0]), __uint_as_float(r[1])); }
DI float xhalf_sum(float x) { auto r = __builtin_amdgcn_permlane32_swap(__float_as_uint(x), __float_as_uint(x), false, false); return __uint_as_float(r[0]) + __uint_as_float(r[1]); }
DI void st_flag(char* p, u64 v) { __hip_atomic_store((GAS u64*)p, v, __ATOMIC_RELAXED, __HIP_MEMORY_SCOPE_AGENT); }
DI u64 ld_flag(const char* p) { return __hip_atomic_load((GAS u64*)p, __ATOMIC_RELAXED, __HIP_MEMORY_SCOPE_AGENT); }
DI void poll(const char* p, bool on, u64 tag) {
#pragma unroll 1
  for (unsigned spin = 0; spin < SPIN; ++spin) {
    const u64 f = on ? ld_flag(p) : tag;
    if (__all(f == tag)) break;
    __builtin_amdgcn_s_sleep(1);
  }
  asm volatile("" ::: "memory");
}

constexpr unsigned KVA = 0, KVB = 65536, KVC = 98304, KVD = 32768;
constexpr unsigned QI1 = 98304, QI2 = 131072;
struct Ring { unsigned b0, b1, b2, w0, w1, w2; DI void rotate() { const unsigned t = b0, u = w0; b0 = b1; b1 = b2; b2 = t; w0 = w1; w1 = w2; w2 = u; } };
constexpr Ring QRING = {118784, 0, 73728, 118784 + 28672, 28672, 73728 + 28672};
constexpr Ring ORING = {16384, 45056, 73728, 131072, 147456, 0};

template <bool ISQ> struct Gemm {
  struct Frag { bf16x8 a[4], b[7]; };
  unsigned abase, bbase;
  DI Gemm(unsigned lds0, unsigned cw) {
    const unsigned lane = threadIdx.x & 63, l4 = lane >> 4, l15 = lane & 15, q4 = (lane >> 2) & 3, pp = lane & 3;
    abase = lds0 + (8 * l4 + q4) * 256 + (((4 * (cw & 1)) ^ ((q4 << 1) | (l4 & 1))) << 5) + pp * 8;
    bbase = lds0 + (cw >> 1) * 14336 + (ISQ ? l15 * 128 + ((l4 ^ (l15 >> 1)) << 4) : lane * 16);
  }
  DI void rd(Frag& f, int i, unsigned bs, unsigned ws, int s) const {
    if (i < 4) { const unsigned a = (abase + ws) ^ (i << 5); f.a[i] = cat(ldstr(a + s * 8192), ldstr(a + s * 8192 + 1024)); }
    else if (i < 11) { const int j = i - 4; f.b[j] = ISQ ? lds128(((bbase + bs) ^ (s << 6)) + j * 2048) : lds128(bbase + bs + s * 7168 + j * 1024); }
  }
  static DI void mm(f32x4 (&acc)[7][4], const Frag& f, int j) {
#pragma unroll
    for (int n = 0; n < 4; ++n) acc[j][n] = mfma16(f.a[n], f.b[j], acc[j][n]);
  }
  DI void run(f32x4 (&acc)[7][4], Ring r) const {
    Frag f0, f1;
#pragma unroll
    for (int i = 0; i < 11; ++i) rd(f0, i, r.b0, r.w0, 0);
    auto half = [&](auto LAST) {
      SB();
#pragma unroll
      for (int j = 0; j < 7; ++j) { mm(acc, f0, j); rd(f1, 2 * j, r.b0, r.w0, 1); rd(f1, 2 * j + 1, r.b0, r.w0, 1); SB(); }
      bar();
      SB();
#pragma unroll
      for (int j = 0; j < 7; ++j) { mm(acc, f1, j); if constexpr (!decltype(LAST)::value) { rd(f0, 2 * j, r.b1, r.w1, 0); rd(f0, 2 * j + 1, r.b1, r.w1, 0); } SB(); }
    };
#pragma unroll 1
    for (unsigned v = 0; v < 15; ++v) { half(IC(0)); r.rotate(); }
    half(IC(1));
  }
};
template <bool ISQ> struct GemmLoad {
  static constexpr unsigned WROW = ISQ ? 8192 : 2048;
  rsrc_t srdB, srdW;
  unsigned voffW, vB0, lds0, lw, hh, p;
  DI GemmLoad(rsrc_t b, rsrc_t w, unsigned lds0_, unsigned lw_, unsigned hh_, unsigned p_) : srdB(b), srdW(w), lds0(lds0_), lw(lw_), hh(hh_), p(p_) {
    const unsigned lane = threadIdx.x & 63, l4 = lane >> 4, l15 = lane & 15;
    voffW = l4 * WROW + ((l15 ^ ((l4 << 2) | (lw & 2))) << 4);
    vB0 = ISQ ? (lane >> 3) * 2048 + ((((lane & 7) ^ l4) << 4) ^ ((lw & 1) << 6)) : lane * 16;
  }
  DI unsigned kk(unsigned v) const {
    if (ISQ) return (v & 8) | ((v + hh + 2 * p) & 7);
    unsigned j = v + ((7 * p) >> 1);
    j = j >= 16 ? j - 14 : j;
    return (2 * hh + (v < 2 ? v : j)) & 15;
  }
  DI void w(unsigned v, unsigned slot) const {
#pragma unroll
    for (unsigned i = 0; i < 4; ++i) dma16<0>(lds0 + slot + lw * 1024, i * 4096, voffW, srdW, (kk(v) * 64 + 4 * lw) * WROW, 16 * i * WROW);
  }
  DI void b1(unsigned v, unsigned slot) const {
#pragma unroll
    for (unsigned i = 0; i < 4; ++i)
      if (i < 3 || lw < 2) dma16<2>(lds0 + slot + lw * 1024, i * 4096, vB0, srdB, kk(v) * 14336 + lw * 1024, i * 4096);
  }
  DI void b(unsigned v, unsigned slot) const {
    if constexpr (ISQ) {
#pragma unroll
      for (unsigned i = 0; i < 7; ++i) dma16<0>(lds0 + slot + lw * 1024, i * 4096, vB0, srdB, lw * 16384 + kk(v) * 128, i * 65536);
    } else {
#pragma unroll
      for (unsigned i = 0; i < 3; ++i) dma16<2>(lds0 + slot + lw * 1024, i * 4096, vB0, srdB, kk(v) * 14336 + lw * 1024, i * 4096);
      const unsigned x = lw < 2 ? 12288 + lw * 1024 : 14336 + 12288 + (3 - lw) * 1024;
      dma16<2>(lds0 + slot + x, 0, vB0, srdB, kk(v) * 14336 + x + (lw < 2 ? 0 : 32 * 28672 - 14336), 0);
#pragma unroll
      for (unsigned i = 0; i < 3; ++i) dma16<2>(lds0 + slot + (3 - lw) * 1024, 14336 + i * 4096, vB0, srdB, kk(v) * 14336 + (3 - lw) * 1024, 32 * 28672 + i * 4096);
    }
  }
  DI void tile(unsigned v, unsigned bs, unsigned ws) const { w(v, ws); b(v, bs); }
};

struct Attn {
  f32x16 o[4], negm, sa, sb;
  bf16x8 qf[8], kf[8], vf[8];
  float mref, lsum;
  unsigned ka[8], va[4];
  DI void rd_k(int i, int ofs) { kf[i] = lds128(ka[i] + ofs); }
  DI void rd_v(int i, int ofs) { const unsigned a = va[i & 3] + ofs + (i >> 2) * 4096; vf[i] = cat(ldstr(a), ldstr(a + 2048)); }
  DI void qk(f32x16& s, int i) {
    if (i == 0) asm("v_mfma_f32_32x32x16_bf16 %0, %1, %2, %3" : "=&v"(s) : "v"(kf[0]), "v"(qf[0]), "v"(negm));
    else s = mfma32(kf[i], qf[i], s);
    if (i == 7) asm volatile("" :: "v"(negm));
  }
  template <bool FIRST> DI void rescale(f32x16& s, float mh) {
    if (FIRST || __builtin_expect(__any(mh > THR), 0)) {
      const float mx = xhalf_max(mh), d = FIRST ? mx : max2f(mx, 0.f);
      mref += d;
#pragma unroll
      for (int i = 0; i < 16; ++i) { s[i] -= d; negm[i] = -mref; }
      if constexpr (!FIRST) {
        const float f = __builtin_amdgcn_exp2f(-d);
        lsum *= f;
#pragma unroll
        for (int c = 0; c < 4; ++c)
#pragma unroll
          for (int i = 0; i < 16; ++i) o[c][i] *= f;
      }
      asm volatile("s_nop 1" : "+v"(negm));
    }
  }
  DI void advance(int delta) {
#pragma unroll
    for (int i = 0; i < 8; ++i) ka[i] += delta;
#pragma unroll
    for (int i = 0; i < 4; ++i) va[i] += delta;
  }
  DI void init(unsigned lds0, unsigned w4, unsigned qimg, unsigned kslot, unsigned slot0) {
    const unsigned lane = threadIdx.x & 63, r32 = lane & 31, h = lane >> 5, l4 = lane >> 4, q4 = (lane >> 2) & 3, pp = lane & 3;
    const unsigned kb = 16384 + r32 * 256 + (((h ^ r32) & 15) << 4);
    const unsigned vb = q4 * 256 + (q4 << 6) + (2 * (l4 & 1) + (pp & 1)) * 16 + (pp >> 1) * 8 + h * 1024;
    const unsigned qr = lds0 + qimg + (32 * w4 + (w4 < 3 ? r32 : lane & 15)) * 256 + (((h ^ r32) & 15) << 4);
#pragma unroll
    for (int i = 0; i < 8; ++i) { ka[i] = lds0 + kslot + (kb ^ (i << 5)); qf[i] = lds128(qr ^ (i << 5)); rd_k(i, 0); SB(); }
#pragma unroll
    for (int c = 0; c < 4; ++c) { o[c] = f32x16{}; va[c] = lds0 + kslot + (vb ^ (c << 6)); }
    negm = f32x16{}; mref = 0.f; lsum = 0.f;
    SB();
    asm volatile("s_nop 1" : "+v"(negm));
#pragma unroll
    for (int i = 0; i < 8; ++i) { qk(sa, i); SB(); }
#pragma unroll
    for (int i = 0; i < 8; ++i) rd_k(i, 8192);
    float m0 = max3f(sa[0], sa[1], sa[2]);
#pragma unroll
    for (int i = 3; i < 15; i += 2) m0 = max3f(m0, sa[i], sa[i + 1]);
    rescale<true>(sa, max2f(m0, sa[15]));
    advance(slot0 - kslot);
    SB();
  }
  template <bool NEXT, bool RDK> DI void sub(f32x16& c, f32x16& n, int ofs) {
    u32x4 p0, p1;
    LGKM(0);
#pragma unroll
    for (int i = 0; i < 8; ++i) {
      if constexpr (NEXT) { qk(n, i); SB(); }
      if (i < 4) rd_v(i, ofs);
      c[2 * i] = __builtin_amdgcn_exp2f(c[2 * i]); c[2 * i + 1] = __builtin_amdgcn_exp2f(c[2 * i + 1]);
      if (i > 0) { const unsigned w = cvtpk(c[2 * i - 2], c[2 * i - 1]); if (i <= 4) p0[i - 1] = w; else p1[i - 5] = w; }
      SB();
    }
    p1[3] = cvtpk(c[14], c[15]);
    LGKM(0);
    SB();
    float m0, m1, l0 = lsum, l1;
#pragma unroll
    for (int i = 0; i < 8; ++i) {
      if (RDK && i == 4) LGKM(7);
      if (RDK && i == 6) LGKM(5);
      o[i & 3] = mfma32(vf[i], __builtin_bit_cast(bf16x8, i < 4 ? p0 : p1), o[i & 3]);
      SB();
      if (i < 4) rd_v(4 + i, ofs);
      if constexpr (RDK) { if (i < 4) rd_k(i, ofs); else if (i < 6) { rd_k(2 * i - 4, ofs); rd_k(2 * i - 3, ofs); } }
      if constexpr (NEXT) {
        if (i == 3) { m0 = max3f(n[0], n[1], n[2]); m1 = max3f(n[3], n[4], n[5]); }
        if (i == 4) { m0 = max3f(m0, n[6], n[7]); m1 = max3f(m1, n[8], n[9]); }
        if (i == 5) { m0 = max3f(m0, n[10], n[11]); m1 = max3f(m1, n[12], n[13]); }
        if (i == 6) { m0 = max3f(m0, n[14], n[15]); }
        if (i == 7) { m0 = max2f(m0, m1); }
      }
      if (i < 2) { l0 = fadd(l0, c[2 * i]); l1 = i ? fadd(l1, c[3]) : c[1]; }
      else if (i < 6) { if (i < 4) { l0 = fadd(l0, c[2 * i]); l1 = fadd(l1, c[2 * i + 1]); } }
      else { l0 = fadd(fadd(l0, c[4 * i - 16]), c[4 * i - 14]); l1 = fadd(fadd(l1, c[4 * i - 15]), c[4 * i - 13]); }
      SB();
    }
    lsum = fadd(l0, l1);
    if constexpr (NEXT) rescale<false>(n, m0);
  }
  DI void run() {
    unsigned s0 = KVA, s1 = KVB, s2 = KVC, s3 = KVD;
    wait_bar<16>();
#pragma unroll 1
    for (unsigned n = 0; n < 15; ++n) {
      sub<true, true>(sa, sb, 0);
      sub<true, true>(sb, sa, 8192);
      advance(s1 - s0);
      const unsigned t = s0; s0 = s1; s1 = s2; s2 = s3; s3 = t;
      if (n == 0) wait_bar<8>(); else wait_bar<0>();
    }
    sub<true, false>(sa, sb, 0);
    sub<false, false>(sb, sa, 8192);
  }
  template <bool TOLDS> DI void store(rsrc_t srdO, unsigned so, unsigned w4, unsigned lds0) {
    const unsigned lane = threadIdx.x & 63, r32 = lane & 31, h = lane >> 5;
    const float inv = 1.f / xhalf_sum(lsum);
    const bool live = w4 < 3 || r32 < 16;
    const unsigned lo = (2 * w4 + (r32 >> 4)) * 1024 + (h * 16 + (lane & 15)) * 16;
    const unsigned vo = live ? so + lo : 0x80000000u;
#pragma unroll
    for (int c = 0; c < 4; ++c) {
      const u32x4 x = pack8(o[c], 0, inv), y = pack8(o[c], 8, inv);
      __builtin_amdgcn_raw_buffer_store_b128(x, srdO, vo + c * 7168, 0, 16);
      __builtin_amdgcn_raw_buffer_store_b128(y, srdO, vo + c * 7168 + 512, 0, 16);
      if (TOLDS && live) {
        const unsigned a = lds0 + lo + ((c >> 1) ? ORING.b1 : ORING.b0) + 14336 + (c & 1) * 7168;
        *(LAS u32x4*)(size_t)a = x; *(LAS u32x4*)(size_t)(a + 512) = y;
      }
    }
  }
};

DI void write_q(const f32x4 (&acc)[7][4], unsigned base, unsigned dh) {
  const unsigned lane = threadIdx.x & 63, l4 = lane >> 4, l15 = lane & 15;
#pragma unroll
  for (int n = 0; n < 4; ++n) {
    const unsigned a = base + l15 * 256 + (l4 & 1) * 8 + (((8 * dh + 2 * n + (l4 >> 1)) ^ l15) << 4);
#pragma unroll
    for (int j = 0; j < 7; ++j) {
      u32x2 w = {cvtpk(acc[j][n][0] * QSCALE, acc[j][n][1] * QSCALE), cvtpk(acc[j][n][2] * QSCALE, acc[j][n][3] * QSCALE)};
      *(LAS u32x2*)(size_t)(a + j * 4096) = w;
    }
  }
}

constexpr int NR = NRT;
constexpr int RT[6] = RTILES;
constexpr int RB[6] = RBURST;
constexpr unsigned mask_of(const int (&a)[6]) { unsigned m = 0; for (int i = 0; i < NR; ++i) m |= 1u << a[i]; return m; }
constexpr unsigned RMASK = mask_of(RT), BMASK = mask_of(RB);

__global__ __launch_bounds__(512, 2)
void k_fused(const u16* __restrict__ xA, const u16* __restrict__ kA, const u16* __restrict__ vA, const u16* __restrict__ wqA, const u16* __restrict__ woA,
             const u16* __restrict__ xB, const u16* __restrict__ kB, const u16* __restrict__ vB, const u16* __restrict__ wqB, const u16* __restrict__ woB,
             char* __restrict__ ws, u16* __restrict__ out) {
  extern __shared__ __attribute__((aligned(16))) char smem[];
  const unsigned lds0 = (unsigned)(size_t)(LAS char*)smem;
  const unsigned lane = threadIdx.x & 63, wid = __builtin_amdgcn_readfirstlane(threadIdx.x >> 6), w4 = wid & 3;
  const unsigned bid = blockIdx.x, xcd = bid & 7, rr = bid >> 3, br = xcd & 1, g = xcd >> 1, hh = rr & 7, p = rr >> 3;
  const unsigned head = 8 * g + hh;
#define KROT 0
  const unsigned krot = KROT;
  STAMP(0, 0); CSTAMP(0, 4);
  const char* const kptr = (const char*)(br ? kB : kA) + (size_t)(2 * p * NH + head) * (SK * DH * 2);
  const char* const vptr = (const char*)(br ? vB : vA) + (size_t)(2 * p * NH + head) * (SK * DH * 2);
  const rsrc_t srdX = mkrsrc((const char*)(br ? xB : xA) + (size_t)p * (2 * SQ * DM * 2), 2 * SQ * DM * 2);
  const rsrc_t srdWq = mkrsrc((const char*)(br ? wqB : wqA) + head * 256, DM * NH * DH * 2 - head * 256);
  const rsrc_t srdWo = mkrsrc((const char*)(br ? woB : woA) + (size_t)g * (1024 * 2048) + hh * 256, 1024 * 2048 - hh * 256);
  const rsrc_t srdO = mkrsrc(ws + WS_O + (size_t)((br * 8 + 2 * p) * 32 + 8 * g) * 28672, (32 + 8) * 28672);
  const rsrc_t srdE = mkrsrc(ws + WS_X, 256 * 131072);
  char* const epoch = ws + WS_E + (br * 4 + p) * 64;
  char* const fam = ws + WS_F + ((br * 4 + g) * 8 + 2 * p) * 2048;
  u16* const outp = out + (size_t)((2 * p + (w4 >> 1)) * SQ) * 2048 + br * 1024 + hh * 128 + (w4 & 1) * 64;
  asm volatile("" :: "s"(kptr), "s"(vptr), "s"(srdX), "s"(srdWq), "s"(srdWo), "s"(srdO), "s"(srdE), "s"(outp));
  u64 tag;
  auto load_tag = [&]() { asm volatile("s_load_dwordx2 %0, %1, 0x0\n\ts_waitcnt lgkmcnt(0)" : "=&s"(tag) : "s"(epoch) : "memory"); tag += 1; };
  const unsigned l4 = lane >> 4, l15 = lane & 15;
  const unsigned voffV = l4 * 256 + ((l15 ^ (l4 << 2)) << 4), voffK = l4 * 256 + ((l15 ^ ((w4 << 2) | l4)) << 4);
  constexpr unsigned KVBYTES = NH * SK * DH * 2 + SK * DH * 2;
  const rsrc_t srdV = mkrsrc(vptr, KVBYTES);
  auto so_k = [&](int n) { return ((n + 1) >> 4) * (NH * SK * DH * 2) + ((n + 1 + krot) & 15) * 16384 + w4 * 1024; };
  auto so_v = [&](int n) { return (n >> 4) * (NH * SK * DH * 2) + ((n + krot) & 15) * 16384 + w4 * 1024; };
  auto issue_k = [&](int n, unsigned slot) {
    const rsrc_t r = mkrsrc(kptr, n < 31 ? KVBYTES : 0);
#pragma unroll
    for (unsigned i = 0; i < 4; ++i) dma16<1>(lds0 + slot + w4 * 1024, 16384 + i * 4096, voffK, r, so_k(n), i * 4096);
  };
  auto issue_kv = [&](int n, unsigned slot) {
    issue_k(n, slot);
#pragma unroll
    for (unsigned i = 0; i < 4; ++i) dma16<1>(lds0 + slot + w4 * 1024, i * 4096, voffV, srdV, so_v(n), i * 4096);
  };
  const unsigned bid0 = bid & ~6u;

  if (wid < 4) {
    Attn at;
    load_tag();
    {
      f32x4 acc[7][4] = {};
      const Gemm<true> gm(lds0, w4);
      rawbar(); rawbar();
      gm.run(acc, QRING);
      write_q(acc, lds0 + ((w4 >> 1) ? QI2 : QI1), w4 & 1);
    }
    bar();
    STAMP(0, 1); CSTAMP(0, 5);
    at.init(lds0, w4, QI1, KVD, KVA);
    at.run();
    STAMP(0, 2); CSTAMP(0, 6);
    const GemmLoad<false> gl(srdO, srdWo, lds0, w4, hh, p);
    bar();
    auto pace = [&]() {};
    at.template store<false>(srdO, hh * 28672, w4, lds0);
    issue_kv(19, KVD);
    {
      unsigned ks = KVA, ks1 = KVB, ks2 = KVC, ks3 = KVD;
      u64 f = 0;
#pragma unroll 1
      for (unsigned n = 17; n < 28; ++n) {
        asm volatile("s_waitcnt vmcnt(16) lgkmcnt(0)\n\ts_barrier" : "+v"(f) :: "memory");
        if (n == 19 && lane == 0) st_flag(fam + (hh * 4 + w4) * 64, tag);
        if (wid == 0) {
          if (n == 24) { const char* a = fam + (lane & 31) * 64; asm volatile("global_load_dwordx2 %0, %1, off sc1" : "+v"(f) : "v"(a) : "memory"); }
          if (n == 26 && !__all(f == tag)) poll(fam + (lane & 31) * 64, true, tag);
        }
        pace();
        issue_kv(n + 3, ks);
        const unsigned t = ks; ks = ks1; ks1 = ks2; ks2 = ks3; ks3 = t;
      }
    }
    wait_bar<16>();
    pace();
    issue_kv(31, KVD);
    gl.w(0, ORING.w0); gl.w(1, ORING.w1);
    wait_bar<24>();
    gl.w(2, ORING.w2);
    wait_bar<20>();
    wait_bar<12>();
    f32x4 acc[7][4] = {};
    const Gemm<false> gm(lds0, w4);
    rawbar();
    STAMP(1, 0);
    gl.b1(0, ORING.b0); gl.b1(1, ORING.b1);
    wait_bar<0>();
    STAMP(1, 2);
    gm.run(acc, ORING);
    STAMP(1, 3);
#pragma unroll
    for (unsigned d = 0; d < 4; ++d)
      if (d != g) {
        const unsigned vo = (bid0 + 2 * d) * 131072 + g * 32768 + w4 * 8192 + lane * 16;
#pragma unroll
        for (int jj = 0; jj < 2; ++jj)
#pragma unroll
          for (int n = 0; n < 4; ++n)
            if (2 * d + jj < 7) __builtin_amdgcn_raw_buffer_store_b128(__builtin_bit_cast(u32x4, acc[2 * d + jj][n]), srdE, vo + (jj * 4 + n) * 1024, 0, 16);
      }
    f32x4 t[2][4];
#pragma unroll
    for (int n = 0; n < 4; ++n) {
      t[0][n] = g == 0 ? acc[0][n] : g == 1 ? acc[2][n] : g == 2 ? acc[4][n] : acc[6][n];
      t[1][n] = g == 0 ? acc[1][n] : g == 1 ? acc[3][n] : acc[5][n];
    }
    wait_vm<0>();
    STAMP(1, 4);
    if (lane < 4) st_flag(ws + WS_G + (((bid0 + 2 * lane) * 4 + w4) * 4 + g) * 64, tag);
    poll(ws + WS_G + ((bid * 4 + w4) * 4 + (lane & 3)) * 64, lane < 4, tag);
    u32x4 e[3][2][4];
#pragma unroll
    for (unsigned sr = 0; sr < 3; ++sr)
#pragma unroll
      for (int jj = 0; jj < 2; ++jj)
#pragma unroll
        for (int n = 0; n < 4; ++n)
          e[sr][jj][n] = __builtin_amdgcn_raw_buffer_load_b128(srdE, (jj && g == 3) ? 0x80000000u : lane * 16 + (jj * 4 + n) * 1024, bid * 131072 + ((g + sr + 1) & 3) * 32768 + w4 * 8192, 16);
#pragma unroll
    for (int jj = 0; jj < 2; ++jj) {
      u16* o = outp + (size_t)((2 * g + jj) * 16 + l15) * 2048 + l4 * 4;
#pragma unroll
      for (int n = 0; n < 4; ++n) {
        const f32x4 v = (t[jj][n] + __builtin_bit_cast(f32x4, e[0][jj][n])) + (__builtin_bit_cast(f32x4, e[1][jj][n]) + __builtin_bit_cast(f32x4, e[2][jj][n]));
        u32x2 w = {cvtpk(v[0], v[1]), cvtpk(v[2], v[3])};
        if (!(jj && g == 3)) *(GAS u32x2*)(o + n * 16) = w;
      }
    }
    if (g == 0 && hh == 0 && wid == 0 && lane == 0) st_flag(epoch, tag);
    STAMP(1, 5); STAMP(1, 6); STAMP(1, 7); STAMP(0, 3);
  } else {
    Attn at;
    {
      u32x4 rk[6][8];
      const rsrc_t srdK = mkrsrc(kptr, KVBYTES);
      auto fetch = [&](auto RI) {
        constexpr int r = decltype(RI)::value;
        if constexpr (r < NR) {
#pragma unroll
          for (int i = 0; i < 4; ++i) ld16nt(rk[r][4 + i], voffK, srdK, so_k(RT[r]), i * 4096);
#pragma unroll
          for (int i = 0; i < 4; ++i) ld16nt(rk[r][i], voffV, srdV, so_v(RT[r]), i * 4096);
        }
      };
      auto put = [&](auto RI, unsigned slot) {
        constexpr int r = decltype(RI)::value;
        if constexpr (r < NR) {
#pragma unroll
          for (int i = 0; i < 8; ++i) *(LAS u32x4*)(size_t)(lds0 + slot + w4 * 1024 + lane * 16 + i * 4096) = rk[r][i];
        }
      };
      const GemmLoad<true> gl(srdX, srdWq, lds0, w4, hh, p);
      Ring r = QRING;
      gl.tile(0, r.b0, r.w0); rawbar();
      gl.tile(1, r.b1, r.w1);
      wait_bar<11>();
      gl.tile(2, r.b2, r.w2);
#pragma unroll
      for (int v = 0; v < 13; ++v) {
        constexpr unsigned BM = BMASK << 2;
        const int nb = ((BM >> v) & 1) + ((BM >> (v + 1)) & 1);
        if (nb == 0) wait_bar<11>(); else if (nb == 1) wait_bar<19>(); else wait_bar<27>();
        gl.tile(v + 3, r.b0, r.w0);
        if ((BMASK >> v) & 1) {
          if (v == RB[0]) fetch(IC(0)); else if (v == RB[1]) fetch(IC(1)); else if (v == RB[2]) fetch(IC(2));
          else if (v == RB[3]) fetch(IC(3)); else if (v == RB[4]) fetch(IC(4)); else fetch(IC(5));
        }
        r.rotate();
      }
      issue_k(-1, KVD);
      wait_bar<15>();
      issue_kv(0, KVA);
      wait_bar<12>();
#pragma unroll
      for (int r = 0; r < NR; ++r) asm volatile("" : "+v"(rk[r][0]), "+v"(rk[r][1]), "+v"(rk[r][2]), "+v"(rk[r][3]), "+v"(rk[r][4]), "+v"(rk[r][5]), "+v"(rk[r][6]), "+v"(rk[r][7]));
      rawbar();
      issue_kv(1, KVB);
      wait_bar<16>();
      STAMP(0, 1); CSTAMP(0, 5);
      wait_bar<8>();
      put(IC(0), KVC);
      unsigned ks = KVD, ks1 = KVA, ks2 = KVB, ks3 = KVC;
#pragma unroll
      for (int n = 0; n < 15; ++n) {
        const int u = n + 3;
        if ((RMASK >> u) & 1) {
          if (u == RT[1]) put(IC(1), ks); else if (u == RT[2]) put(IC(2), ks); else if (u == RT[3]) put(IC(3), ks);
          else if (u == RT[4]) put(IC(4), ks); else put(IC(5), ks);
        } else issue_kv(u, ks);
        const unsigned t = ks; ks = ks1; ks1 = ks2; ks2 = ks3; ks3 = t;
        const int nd = 2 - ((RMASK >> (n + 2)) & 1) - ((RMASK >> u) & 1);
        if (nd == 2) wait_bar<16>(); else if (nd == 1) wait_bar<8>(); else wait_bar<0>();
      }
      issue_kv(18, KVC);
    }
    load_tag();
    at.init(lds0, w4, QI2, KVD, KVA);
    STAMP(0, 2); CSTAMP(0, 6);
    at.run();
    STAMP(1, 0);
    bar();
    at.template store<true>(srdO, (32 + hh) * 28672, w4, lds0);
    const GemmLoad<false> gl(srdO, srdWo, lds0, w4, hh, p);
    bar();
    rawbar();
    Ring r = ORING;
    gl.w(3, r.w0);
    wait_vm<4>();
    if (lane == 0) st_flag(fam + 2048 + (hh * 4 + w4) * 64, tag);
    STAMP(1, 1);
    poll(fam + 2048 + (lane & 31) * 64, true, tag);
    STAMP(1, 2);
    gl.b(2, r.b2); gl.b(3, r.b0);
    wait_bar<7>();
    r.rotate();
    gl.tile(4, r.b0, r.w0);
    r.rotate();
#pragma unroll 1
    for (unsigned v = 2; v < 13; ++v) { wait_bar<11>(); gl.tile(v + 3, r.b0, r.w0); r.rotate(); }
    wait_bar<11>();
    wait_bar<0>();
    rawbar();
    STAMP(1, 3); STAMP(1, 4);
    STAMP(1, 5); STAMP(1, 6); STAMP(1, 7); STAMP(0, 3);
  }
}

extern "C" void kernel_launch(void* const* d_in, const int* in_sizes, int n_in, void* d_out, int out_size, void* d_ws, size_t ws_size, hipStream_t stream) {
  (void)hipFuncSetAttribute((const void*)k_fused, hipFuncAttributeMaxDynamicSharedMemorySize, 163840);
  k_fused<<<256, 512, 163840, stream>>>((const u16*)d_in[0], (const u16*)d_in[1], (const u16*)d_in[2], (const u16*)d_in[3], (const u16*)d_in[4],
                                        (const u16*)d_in[5], (const u16*)d_in[6], (const u16*)d_in[7], (const u16*)d_in[8], (const u16*)d_in[9],
                                        (char*)d_ws, (u16*)d_out);
}
```

```cpp
#include <hip/hip_runtime.h>
#include <stdint.h>
#include <type_traits>

typedef unsigned short u16;
typedef unsigned long long u64;
typedef __bf16 bf16x8 __attribute__((ext_vector_type(8)));
typedef __bf16 bf16x4 __attribute__((ext_vector_type(4)));
typedef __bf16 bf16x2 __attribute__((ext_vector_type(2)));
typedef float f32x16 __attribute__((ext_vector_type(16)));
typedef float f32x4 __attribute__((ext_vector_type(4)));
typedef float f32x2 __attribute__((ext_vector_type(2)));
typedef unsigned u32x4 __attribute__((ext_vector_type(4)));
typedef unsigned u32x2 __attribute__((ext_vector_type(2)));
#define LAS __attribute__((address_space(3)))
#define GAS __attribute__((address_space(1)))
#define DI __device__ __forceinline__
#define SB() __builtin_amdgcn_sched_barrier(0)
#define LGKM(n) do { __builtin_amdgcn_s_waitcnt(0xC07F | ((n) << 8)); SB(); } while (0)
#define IC(n) std::integral_constant<int, n>{}
typedef __amdgpu_buffer_rsrc_t rsrc_t;

constexpr int SQ = 112, DM = 1024, NH = 32, SK = 1024, DH = 128;
constexpr size_t WS_O = 0;
constexpr size_t WS_X = (size_t)16 << 20;
constexpr size_t WS_F = (size_t)48 << 20;
constexpr size_t WS_G = WS_F + 131072;
constexpr size_t WS_E = WS_G + 524288;
constexpr size_t WS_ST = (size_t)64 << 20;
constexpr float QSCALE = 0.08838834764831845f * 1.4426950408889634f;
constexpr float THR = 8.0f;
constexpr unsigned SPIN = 1u << 18;

#define NOUT1 24
#define NOUT2 24
#define MGR 2
#define SLP1 0
#define RDN 22
#define QW 8
#define NRT 4
#define RTILES {2, 6, 10, 13, 0, 0}
#define RBURST {2, 5, 8, 11, 99, 99}
#define CSTAMP(kern, idx) do {} while (0)
#define STAMP(kern, idx) do {} while (0)

DI rsrc_t mkrsrc(const void* p, unsigned bytes) { return __builtin_amdgcn_make_buffer_rsrc((void*)p, (short)0, (int)bytes, 0x00020000); }
template <int POL> DI void dma16(unsigned mb, unsigned mi, unsigned voff, rsrc_t srd, unsigned sb, unsigned si) {
  unsigned t;
  if constexpr (POL == 1)      asm volatile("s_add_u32 m0, %1, %2\n\ts_add_u32 %0, %5, %6\n\tbuffer_load_dwordx4 %3, %4, %0 offen nt lds"  : "=&s"(t) : "s"(mb), "n"(mi), "v"(voff), "s"(srd), "s"(sb), "n"(si) : "memory", "scc");
  else if constexpr (POL == 2) asm volatile("s_add_u32 m0, %1, %2\n\ts_add_u32 %0, %5, %6\n\tbuffer_load_dwordx4 %3, %4, %0 offen sc1 lds" : "=&s"(t) : "s"(mb), "n"(mi), "v"(voff), "s"(srd), "s"(sb), "n"(si) : "memory", "scc");
  else                         asm volatile("s_add_u32 m0, %1, %2\n\ts_add_u32 %0, %5, %6\n\tbuffer_load_dwordx4 %3, %4, %0 offen lds"     : "=&s"(t) : "s"(mb), "n"(mi), "v"(voff), "s"(srd), "s"(sb), "n"(si) : "memory", "scc");
}
DI void ld16nt(u32x4& d, unsigned voff, rsrc_t srd, unsigned sb, unsigned si) {
  unsigned t;
  asm volatile("s_add_u32 %1, %4, %5\n\tbuffer_load_dwordx4 %0, %2, %3, %1 offen nt" : "=v"(d), "=&s"(t) : "v"(voff), "s"(srd), "s"(sb), "n"(si) : "memory", "scc");
}
DI void landed(u32x4 (&r)[8]) { asm volatile("" : "+v"(r[0]), "+v"(r[1]), "+v"(r[2]), "+v"(r[3]), "+v"(r[4]), "+v"(r[5]), "+v"(r[6]), "+v"(r[7])); }
template <int N> DI void wait_bar() { asm volatile("s_waitcnt vmcnt(%0) lgkmcnt(0)\n\ts_barrier" ::"n"(N) : "memory"); }
template <int N> DI void wait_vm() { asm volatile("s_waitcnt vmcnt(%0)" ::"n"(N) : "memory"); }
DI void bar() { asm volatile("s_waitcnt lgkmcnt(0)\n\ts_barrier" ::: "memory"); }
DI void rawbar() { asm volatile("s_barrier" ::: "memory"); }
DI bf16x8 lds128(unsigned a) { return *(const LAS bf16x8*)(size_t)a; }
DI bf16x4 ldstr(unsigned a) { return __builtin_amdgcn_ds_read_tr16_b64_v4bf16((LAS bf16x4*)(size_t)a); }
DI bf16x8 cat(bf16x4 a, bf16x4 b) { return __builtin_shufflevector(a, b, 0, 1, 2, 3, 4, 5, 6, 7); }
DI f32x16 mfma32(bf16x8 a, bf16x8 b, f32x16 c) { return __builtin_amdgcn_mfma_f32_32x32x16_bf16(a, b, c, 0, 0, 0); }
DI f32x4 mfma16(bf16x8 a, bf16x8 b, f32x4 c) { return __builtin_amdgcn_mfma_f32_16x16x32_bf16(a, b, c, 0, 0, 0); }
DI void mac16(f32x4& c, bf16x8 a, bf16x8 b) { asm("v_mfma_f32_16x16x32_bf16 %0, %1, %2, %0" : "+v"(c) : "v"(a), "v"(b)); }
DI unsigned cvtpk(float lo, float hi) { f32x2 v = {lo, hi}; return __builtin_bit_cast(unsigned, __builtin_convertvector(v, bf16x2)); }
DI u32x4 pack8(const f32x16& a, int o, float s) {
  u32x4 w = {cvtpk(a[o] * s, a[o + 1] * s), cvtpk(a[o + 2] * s, a[o + 3] * s), cvtpk(a[o + 4] * s, a[o + 5] * s), cvtpk(a[o + 6] * s, a[o + 7] * s)};
  return w;
}
DI float fadd(float a, float b) { float r; asm("v_add_f32_e32 %0, %1, %2" : "=v"(r) : "v"(a), "v"(b)); return r; }
DI float max2f(float a, float b) { return __builtin_elementwise_maximum(a, b); }
DI float max3f(float a, float b, float c) { return max2f(max2f(a, b), c); }
DI float xrow_max(float x) {
  auto a = __builtin_amdgcn_permlane16_swap(__float_as_uint(x), __float_as_uint(x), false, false);
  x = max2f(__uint_as_float(a[0]), __uint_as_float(a[1]));
  auto b = __builtin_amdgcn_permlane32_swap(__float_as_uint(x), __float_as_uint(x), false, false);
  return max2f(__uint_as_float(b[0]), __uint_as_float(b[1]));
}
DI float xhalf_max(float x) { auto r = __builtin_amdgcn_permlane32_swap(__float_as_uint(x), __float_as_uint(x), false, false); return max2f(__uint_as_float(r[0]), __uint_as_float(r[1])); }
DI float xhalf_sum(float x) { auto r = __builtin_amdgcn_permlane32_swap(__float_as_uint(x), __float_as_uint(x), false, false); return __uint_as_float(r[0]) + __uint_as_float(r[1]); }
DI void st_flag(char* p, u64 v) { __hip_atomic_store((GAS u64*)p, v, __ATOMIC_RELAXED, __HIP_MEMORY_SCOPE_AGENT); }
DI u64 ld_flag(const char* p) { return __hip_atomic_load((GAS u64*)p, __ATOMIC_RELAXED, __HIP_MEMORY_SCOPE_AGENT); }
DI void poll(const char* p, bool on, u64 tag) {
#pragma unroll 1
  for (unsigned spin = 0; spin < SPIN; ++spin) {
    const u64 f = on ? ld_flag(p) : tag;
    if (__all(f == tag)) break;
    __builtin_amdgcn_s_sleep(1);
  }
  asm volatile("" ::: "memory");
}

constexpr unsigned KVA = 0, KVB = 65536, KVC = 98304, KVD = 32768;
constexpr unsigned QI1 = 98304, QI2 = 131072;
struct Ring { unsigned b0, b1, b2, w0, w1, w2; DI void rotate() { const unsigned t = b0, u = w0; b0 = b1; b1 = b2; b2 = t; w0 = w1; w1 = w2; w2 = u; } };
constexpr Ring QRING = {118784, 0, 73728, 118784 + 28672, 28672, 73728 + 28672};
constexpr Ring ORING = {16384, 73728, 45056, 131072, 147456, 0};

template <bool ISQ> struct Gemm {
  struct Frag { bf16x8 a[4], b[7]; };
  unsigned abase, bbase;
  DI Gemm(unsigned lds0, unsigned cw) {
    const unsigned lane = threadIdx.x & 63, l4 = lane >> 4, l15 = lane & 15, q4 = (lane >> 2) & 3, pp = lane & 3;
    abase = lds0 + (ISQ ? 8 * l4 + q4 : 4 * l4 + q4) * 256 + (((4 * (cw & 1)) ^ (ISQ ? (q4 << 1) | (l4 & 1) : 4 * (l4 & 1) + q4)) << 5) + pp * 8;
    bbase = lds0 + (cw >> 1) * 14336 + (ISQ ? l15 * 128 + ((l4 ^ (l15 >> 1)) << 4) : lane * 16);
  }
  struct Half { bf16x4 a[8]; };
  DI void rd(Frag& f, Half& h, int i, unsigned bs, unsigned ws, int s) const {
    if (i < 8) { h.a[i] = ldstr(((abase + ws) ^ ((i >> 1) << 5)) + s * 8192 + (i & 1) * (ISQ ? 1024 : 4096)); if (i & 1) f.a[i >> 1] = cat(h.a[i - 1], h.a[i]); }
    else if (i < 15) { const int j = i - 8; f.b[j] = ISQ ? lds128(((bbase + bs) ^ (s << 6)) + j * 2048) : lds128(bbase + bs + s * 7168 + j * 1024); }
  }
  template <bool RD> DI void half(f32x4 (&acc)[7][4], const Frag& c, Frag& n, unsigned bs, unsigned ws, int s) const {
    Half h;
#pragma unroll
    for (int m = 0; m < 28; ++m) {
      acc[m >> 2][m & 3] = mfma16(c.a[m & 3], c.b[m >> 2], acc[m >> 2][m & 3]);
      SB();
      if constexpr (RD) { if (m < RDN) { if (RDN == 15) rd(n, h, m, bs, ws, s); else if ((m % 3) != 2) rd(n, h, m - m / 3, bs, ws, s); } SB(); }
    }
  }
  DI void run(f32x4 (&acc)[7][4], Ring r) const {
    Frag f0, f1;
    {
      Half h;
#pragma unroll
      for (int i = 0; i < 15; ++i) rd(f0, h, i, r.b0, r.w0, 0);
    }
#pragma unroll 1
    for (unsigned v = 0; v < 15; ++v) {
      LGKM(0);
      half<true>(acc, f0, f1, r.b0, r.w0, 1);
      LGKM(0); rawbar(); SB();
      half<true>(acc, f1, f0, r.b1, r.w1, 0);
      r.rotate();
    }
    LGKM(0);
    half<true>(acc, f0, f1, r.b0, r.w0, 1);
    LGKM(0); rawbar(); SB();
    half<false>(acc, f1, f0, 0, 0, 0);
  }
};
template <bool ISQ> struct GemmLoad {
  static constexpr unsigned WROW = ISQ ? 8192 : 2048;
  rsrc_t srdB, srdW;
  unsigned voffW, vB0, lds0, lw, hh, p;
  DI GemmLoad(rsrc_t b, rsrc_t w, unsigned lds0_, unsigned lw_, unsigned hh_, unsigned p_) : srdB(b), srdW(w), lds0(lds0_), lw(lw_), hh(hh_), p(p_) {
    const unsigned lane = threadIdx.x & 63, l4 = lane >> 4, l15 = lane & 15;
    voffW = l4 * WROW + ((l15 ^ (ISQ ? (l4 << 2) | (lw & 2) : (((lw & 1) << 2) | l4) << 1)) << 4);
    vB0 = ISQ ? (lane >> 3) * 2048 + ((((lane & 7) ^ l4) << 4) ^ ((lw & 1) << 6)) : lane * 16;
  }
  DI unsigned kk(unsigned v) const {
    if (ISQ) return (v & 8) | ((v + hh + 2 * p) & 7);
    unsigned j = v + ((7 * p) >> 1);
    j = j >= 16 ? j - 14 : j;
    return (2 * hh + (v < 2 ? v : j)) & 15;
  }
  DI void w(unsigned v, unsigned slot) const {
#pragma unroll
    for (unsigned i = 0; i < 4; ++i) dma16<0>(lds0 + slot + lw * 1024, i * 4096, voffW, srdW, (kk(v) * 64 + 4 * lw) * WROW, 16 * i * WROW);
  }
  DI void b(unsigned v, unsigned slot) const {
    if constexpr (ISQ) {
#pragma unroll
      for (unsigned i = 0; i < 7; ++i) dma16<0>(lds0 + slot + lw * 1024, i * 4096, vB0, srdB, lw * 16384 + kk(v) * 128, i * 65536);
    } else {
#pragma unroll
      for (unsigned i = 0; i < 3; ++i) dma16<2>(lds0 + slot + lw * 1024, i * 4096, vB0, srdB, kk(v) * 14336 + lw * 1024, i * 4096);
      const unsigned x = lw < 2 ? 12288 + lw * 1024 : 14336 + 12288 + (3 - lw) * 1024;
      dma16<2>(lds0 + slot + x, 0, vB0, srdB, kk(v) * 14336 + x + (lw < 2 ? 0 : 32 * 28672 - 14336), 0);
#pragma unroll
      for (unsigned i = 0; i < 3; ++i) dma16<2>(lds0 + slot + (3 - lw) * 1024, 14336 + i * 4096, vB0, srdB, kk(v) * 14336 + (3 - lw) * 1024, 32 * 28672 + i * 4096);
    }
  }
  DI void tile(unsigned v, unsigned bs, unsigned ws) const { w(v, ws); b(v, bs); }
};

struct Score { f32x4 s[2][2]; };
struct Attn {
  f32x4 o[8][2], ls[2], negm[2];
  Score sa, sb;
  bf16x8 qf[4][2], kf[8], vf[8], ones;
  bf16x4 vh;
  float mref[2];
  unsigned ka[4], va[8];
  DI void rd_k(int f, int ofs) { kf[f] = lds128(ka[f >> 1] + ofs + (f & 1) * 4096); }
  DI void rd_v(int i, int ofs) { const bf16x4 x = ldstr(va[i >> 1] + ofs + (i & 1) * 4096); if (i & 1) vf[i >> 1] = cat(vh, x); else vh = x; }
  DI void qk(Score& n, int i) {
    const int f = i >> 1, b = i & 1;
    if (f < 2) asm("v_mfma_f32_16x16x32_bf16 %0, %1, %2, %3" : "=&v"(n.s[f][b]) : "v"(kf[f]), "v"(qf[0][b]), "v"(negm[b]));
    else mac16(n.s[f & 1][b], kf[f], qf[f >> 1][b]);
    if (i == 7) asm volatile("" :: "v"(negm[0]), "v"(negm[1]));
  }
  DI void settle() {
    asm volatile("s_nop 7" : "+v"(o[0][0]), "+v"(o[0][1]), "+v"(o[1][0]), "+v"(o[1][1]), "+v"(o[2][0]), "+v"(o[2][1]), "+v"(o[3][0]), "+v"(o[3][1]), "+v"(o[4][0]), "+v"(o[4][1]),
                 "+v"(o[5][0]), "+v"(o[5][1]), "+v"(o[6][0]), "+v"(o[6][1]), "+v"(o[7][0]), "+v"(o[7][1]), "+v"(ls[0]), "+v"(ls[1]));
  }
  template <bool FIRST> DI void rescale(Score& n, float mall, bool pad) {
    if (FIRST || __builtin_expect(__any(mall > THR), 0)) {
      if constexpr (!FIRST) settle();
#pragma unroll
      for (int b = 0; b < 2; ++b) {
        const f32x4 &x = n.s[0][b], &y = n.s[1][b];
        const float mx = xrow_max(max2f(max3f(max3f(x[0], x[1], x[2]), x[3], y[0]), max3f(y[1], y[2], y[3])));
        const float d = FIRST ? ((b && pad) ? 1e30f : mx) : max2f(mx, 0.f);
        mref[b] += d;
        n.s[0][b] -= d; n.s[1][b] -= d;
        negm[b] = f32x4{-mref[b], -mref[b], -mref[b], -mref[b]};
        if constexpr (!FIRST) {
          const float f = __builtin_amdgcn_exp2f(-d);
          ls[b] *= f;
#pragma unroll
          for (int c = 0; c < 8; ++c) o[c][b] *= f;
        }
      }
      asm volatile("s_nop 1" : "+v"(negm[0]), "+v"(negm[1]));
    }
  }
  DI void advance(int delta) {
#pragma unroll
    for (int i = 0; i < 4; ++i) ka[i] += delta;
#pragma unroll
    for (int i = 0; i < 8; ++i) va[i] += delta;
  }
  DI void init(unsigned lds0, unsigned w4, unsigned qimg, unsigned kslot, unsigned slot0) {
    const unsigned lane = threadIdx.x & 63, l15 = lane & 15, l4 = lane >> 4, q4 = (lane >> 2) & 3, pp = lane & 3;
    const unsigned kb = l15 * 256 + ((l4 ^ l15) << 4);
    const unsigned vb = (4 * l4 + q4) * 256 + ((4 * (l4 & 1) + q4) << 5) + pp * 8;
    const bool pad = w4 == 3;
#pragma unroll
    for (int i = 0; i < 4; ++i) {
      ka[i] = lds0 + kslot + 16384 + (kb ^ (i << 6));
      const unsigned a = lds0 + qimg + w4 * 8192 + (kb ^ (i << 6));
      qf[i][0] = lds128(a); qf[i][1] = lds128(a + (pad ? 0 : 4096));
      rd_k(2 * i, 0); rd_k(2 * i + 1, 0);
      SB();
    }
#pragma unroll
    for (int c = 0; c < 8; ++c) { o[c][0] = f32x4{}; o[c][1] = f32x4{}; va[c] = lds0 + kslot + (vb ^ (c << 5)); }
    if (pad) {
#pragma unroll
      for (int i = 0; i < 4; ++i) qf[i][1] = bf16x8{};
    }
    ones = __builtin_bit_cast(bf16x8, u32x4{0x3f803f80u, 0x3f803f80u, 0x3f803f80u, 0x3f803f80u});
    ls[0] = ls[1] = negm[0] = negm[1] = f32x4{}; mref[0] = mref[1] = 0.f;
    SB();
    asm volatile("s_nop 1" : "+v"(negm[0]), "+v"(negm[1]), "+v"(ones), "+v"(qf[0][1]));
#pragma unroll
    for (int i = 0; i < 16; ++i) { qk(sa, i); SB(); }
#pragma unroll
    for (int f = 0; f < 8; ++f) rd_k(f, 8192);
    asm volatile("s_nop 7" : "+v"(sa.s[0][0]), "+v"(sa.s[0][1]), "+v"(sa.s[1][0]), "+v"(sa.s[1][1]));
    rescale<true>(sa, 0.f, pad);
    advance(slot0 - kslot);
    SB();
  }
  template <bool NEXT, bool RDK> DI void sub(Score& c, Score& n, int ofs) {
    u32x4 p[2];
    LGKM(0);
#pragma unroll
    for (int i = 0; i < 16; ++i) {
      if constexpr (NEXT) { qk(n, i); SB(); }
      f32x4& x = c.s[(i >> 2) & 1][i >> 3];
      x[i & 3] = __builtin_amdgcn_exp2f(x[i & 3]);
      if (i >= 2 && !(i & 1)) { const int k = i - 2; const f32x4& y = c.s[(k >> 2) & 1][k >> 3]; p[k >> 3][(k >> 1) & 3] = cvtpk(y[k & 3], y[(k & 3) + 1]); }
      if (i >= 8) rd_v(i - 8, ofs);
      SB();
    }
    p[1][3] = cvtpk(c.s[1][1][2], c.s[1][1][3]);
    SB();
    float m0, m1;
#pragma unroll
    for (int i = 0; i < 18; ++i) {
      if (i == 0 || i == 4) LGKM(4);
      if (RDK && i == 8) LGKM(8);
      if (RDK && i == 12) LGKM(5);
      if (i < 16) mac16(o[i >> 1][i & 1], vf[i >> 1], __builtin_bit_cast(bf16x8, p[i & 1]));
      else mac16(ls[i & 1], ones, __builtin_bit_cast(bf16x8, p[i & 1]));
      SB();
      if (i < 8) rd_v(8 + i, ofs);
      if constexpr (RDK) { if (i >= 4 && i < 12) rd_k(i - 4, ofs); }
      if constexpr (NEXT) {
        if (i == 2) m0 = max3f(n.s[0][0][0], n.s[0][0][1], n.s[0][0][2]);
        if (i == 3) m1 = max3f(n.s[0][0][3], n.s[0][1][0], n.s[0][1][1]);
        if (i == 11) m0 = max3f(m0, n.s[0][1][2], n.s[0][1][3]);
        if (i == 12) m1 = max3f(m1, n.s[1][0][0], n.s[1][0][1]);
        if (i == 13) m0 = max3f(m0, n.s[1][0][2], n.s[1][0][3]);
        if (i == 14) m1 = max3f(m1, n.s[1][1][0], n.s[1][1][1]);
        if (i == 15) m0 = max3f(m0, n.s[1][1][2], n.s[1][1][3]);
        if (i == 16) m0 = max2f(m0, m1);
      }
      SB();
    }
    if constexpr (NEXT) rescale<false>(n, m0, false);
  }
  template <int SLP> DI void run() {
    unsigned s0 = KVA, s1 = KVB, s2 = KVC, s3 = KVD;
    wait_bar<8>();
#pragma unroll 1
    for (unsigned n = 0; n < 15; ++n) {
      sub<true, true>(sa, sb, 0);
      sub<true, true>(sb, sa, 8192);
      advance(s1 - s0);
      if (SLP) __builtin_amdgcn_s_sleep(SLP);
      const unsigned t = s0; s0 = s1; s1 = s2; s2 = s3; s3 = t;
      wait_bar<0>();
    }
    sub<true, false>(sa, sb, 0);
    sub<false, false>(sb, sa, 8192);
    settle();
  }
  DI void store(u32x4 (&of)[8], rsrc_t srdO, unsigned so, unsigned w4) {
    const unsigned vo = so + 2 * w4 * 1024 + (threadIdx.x & 63) * 16;
#pragma unroll
    for (int b = 0; b < 2; ++b) {
      const float inv = 1.f / ls[b][0];
#pragma unroll
      for (int s = 0; s < 4; ++s) {
        const f32x4 x = o[2 * s][b] * inv, y = o[2 * s + 1][b] * inv;
        of[2 * s + b] = u32x4{cvtpk(x[0], x[1]), cvtpk(x[2], x[3]), cvtpk(y[0], y[1]), cvtpk(y[2], y[3])};
        __builtin_amdgcn_raw_buffer_store_b128(of[2 * s + b], srdO, (b && w4 == 3) ? 0x80000000u : vo + s * 7168 + b * 1024, 0, 16);
      }
    }
  }
};
template <int T> DI void put_o(const u32x4 (&of)[8], unsigned base, unsigned w4) {
  const unsigned a = base + 2 * w4 * 1024 + (threadIdx.x & 63) * 16;
  *(LAS u32x4*)(size_t)a = of[4 * T]; *(LAS u32x4*)(size_t)(a + 7168) = of[4 * T + 2];
  if (w4 < 3) { *(LAS u32x4*)(size_t)(a + 1024) = of[4 * T + 1]; *(LAS u32x4*)(size_t)(a + 8192) = of[4 * T + 3]; }
}

DI void write_q(const f32x4 (&acc)[7][4], unsigned base, unsigned dh) {
  const unsigned lane = threadIdx.x & 63, l4 = lane >> 4, l15 = lane & 15;
#pragma unroll
  for (int n = 0; n < 4; ++n) {
    const unsigned a = base + l15 * 256 + (l4 & 1) * 8 + (((8 * dh + 2 * n + (l4 >> 1)) ^ l15) << 4);
#pragma unroll
    for (int j = 0; j < 7; ++j) {
      u32x2 w = {cvtpk(acc[j][n][0] * QSCALE, acc[j][n][1] * QSCALE), cvtpk(acc[j][n][2] * QSCALE, acc[j][n][3] * QSCALE)};
      *(LAS u32x2*)(size_t)(a + j * 4096) = w;
    }
  }
}

constexpr int NR = NRT;
constexpr int RT[6] = RTILES;
constexpr int RB[6] = RBURST;
constexpr unsigned mask_of(const int (&a)[6]) { unsigned m = 0; for (int i = 0; i < NR; ++i) m |= 1u << a[i]; return m; }
constexpr unsigned RMASK = mask_of(RT), BMASK = mask_of(RB);

__global__ __launch_bounds__(512, 2)
void k_fused(const u16* __restrict__ xA, const u16* __restrict__ kA, const u16* __restrict__ vA, const u16* __restrict__ wqA, const u16* __restrict__ woA,
             const u16* __restrict__ xB, const u16* __restrict__ kB, const u16* __restrict__ vB, const u16* __restrict__ wqB, const u16* __restrict__ woB,
             char* __restrict__ ws, u16* __restrict__ out) {
  extern __shared__ __attribute__((aligned(16))) char smem[];
  const unsigned lds0 = (unsigned)(size_t)(LAS char*)smem;
  const unsigned lane = threadIdx.x & 63, wid = __builtin_amdgcn_readfirstlane(threadIdx.x >> 6), w4 = wid & 3;
  const unsigned bid = blockIdx.x, xcd = bid & 7, rr = bid >> 3, br = xcd & 1, g = xcd >> 1, hh = rr & 7, p = rr >> 3;
  const unsigned head = 8 * g + hh;
#define KROT 0
  const unsigned krot = KROT;
  STAMP(0, 0); CSTAMP(0, 4);
  const char* const kptr = (const char*)(br ? kB : kA) + (size_t)(2 * p * NH + head) * (SK * DH * 2);
  const char* const vptr = (const char*)(br ? vB : vA) + (size_t)(2 * p * NH + head) * (SK * DH * 2);
  const rsrc_t srdX = mkrsrc((const char*)(br ? xB : xA) + (size_t)p * (2 * SQ * DM * 2), 2 * SQ * DM * 2);
  const rsrc_t srdWq = mkrsrc((const char*)(br ? wqB : wqA) + head * 256, DM * NH * DH * 2 - head * 256);
  const rsrc_t srdWo = mkrsrc((const char*)(br ? woB : woA) + (size_t)g * (1024 * 2048) + hh * 256, 1024 * 2048 - hh * 256);
  const rsrc_t srdO = mkrsrc(ws + WS_O + (size_t)((br * 8 + 2 * p) * 32 + 8 * g) * 28672, (32 + 8) * 28672);
  const rsrc_t srdE = mkrsrc(ws + WS_X, 256 * 65536);
  char* const epoch = ws + WS_E + (br * 4 + p) * 64;
  char* const fam = ws + WS_F + ((br * 4 + g) * 8 + 2 * p) * 2048;
  u16* const outp = out + (size_t)((2 * p + (w4 >> 1)) * SQ) * 2048 + br * 1024 + hh * 128 + (w4 & 1) * 64;
  asm volatile("" :: "s"(kptr), "s"(vptr), "s"(srdX), "s"(srdWq), "s"(srdWo), "s"(srdO), "s"(srdE), "s"(outp));
  u64 tag;
  auto load_tag = [&]() { asm volatile("s_load_dwordx2 %0, %1, 0x0\n\ts_waitcnt lgkmcnt(0)" : "=&s"(tag) : "s"(epoch) : "memory"); tag += 1; };
  const unsigned l4 = lane >> 4, l15 = lane & 15;
  const unsigned voffV = l4 * 256 + ((l15 ^ ((((w4 & 1) << 2) | l4) << 1)) << 4), voffK = l4 * 256 + ((l15 ^ ((w4 << 2) | l4)) << 4);
  constexpr unsigned KVBYTES = NH * SK * DH * 2 + SK * DH * 2;
  const rsrc_t srdV = mkrsrc(vptr, KVBYTES);
  auto so_k = [&](int n) { return ((n + 1) >> 4) * (NH * SK * DH * 2) + ((n + 1 + krot) & 15) * 16384 + w4 * 1024; };
  auto so_v = [&](int n) { return (n >> 4) * (NH * SK * DH * 2) + ((n + krot) & 15) * 16384 + w4 * 1024; };
  auto issue_k = [&](int n, unsigned slot) {
    const rsrc_t r = mkrsrc(kptr, n < 31 ? KVBYTES : 0);
#pragma unroll
    for (unsigned i = 0; i < 4; ++i) dma16<1>(lds0 + slot + w4 * 1024, 16384 + i * 4096, voffK, r, so_k(n), i * 4096);
  };
  auto issue_kv = [&](int n, unsigned slot) {
    issue_k(n, slot);
#pragma unroll
    for (unsigned i = 0; i < 4; ++i) dma16<1>(lds0 + slot + w4 * 1024, i * 4096, voffV, srdV, so_v(n), i * 4096);
  };
  auto issue_kv_m = [&](auto NO, int n, unsigned slot) {
    constexpr int N = decltype(NO)::value;
    const rsrc_t r = mkrsrc(kptr, n < 31 ? KVBYTES : 0);
#pragma unroll
    for (unsigned i = 0; i < 4; ++i) { if (i % MGR == 0) wait_vm<N - MGR>(); dma16<1>(lds0 + slot + w4 * 1024, 16384 + i * 4096, voffK, r, so_k(n), i * 4096); }
#pragma unroll
    for (unsigned i = 0; i < 4; ++i) { if (i % MGR == 0) wait_vm<N - MGR>(); dma16<1>(lds0 + slot + w4 * 1024, i * 4096, voffV, srdV, so_v(n), i * 4096); }
  };
  const unsigned bid0 = bid & ~6u;

  if (wid < 4) {
    Attn at;
    load_tag();
    {
      f32x4 acc[7][4] = {};
      const Gemm<true> gm(lds0, w4);
      rawbar(); rawbar();
      gm.run(acc, QRING);
      write_q(acc, lds0 + ((w4 >> 1) ? QI2 : QI1), w4 & 1);
    }
    bar();
    STAMP(0, 1); CSTAMP(0, 5);
    at.init(lds0, w4, QI1, KVD, KVA);
    at.template run<SLP1>();
    STAMP(0, 2); CSTAMP(0, 6);
    const GemmLoad<false> gl(srdO, srdWo, lds0, w4, hh, p);
    bar();
    issue_kv(18, KVC);
    u32x4 of[8];
    at.store(of, srdO, hh * 28672, w4);
    rawbar();
    issue_kv(19, KVD);
    wait_bar<16>();
    issue_kv_m(IC(NOUT2), 20, KVA);
    {
      unsigned ks = KVB, ks1 = KVC, ks2 = KVD, ks3 = KVA;
      u64 f = 0;
#pragma unroll 1
      for (unsigned n = 19; n < 29; ++n) {
        asm volatile("s_waitcnt vmcnt(8) lgkmcnt(0)\n\ts_barrier" : "+v"(f) :: "memory");
        if (n == 19 && lane == 0) st_flag(fam + (hh * 4 + w4) * 64, tag);
        if (wid == 0) {
          if (n == 25) { const char* a = fam + (lane & 31) * 64; asm volatile("global_load_dwordx2 %0, %1, off sc1" : "+v"(f) : "v"(a) : "memory"); }
          if (n == 26 && !__all(f == tag)) poll(fam + (lane & 31) * 64, true, tag);
        }
        issue_kv_m(IC(NOUT2), n + 2, ks);
        const unsigned t = ks; ks = ks1; ks1 = ks2; ks2 = ks3; ks3 = t;
      }
    }
    wait_bar<8>();
    issue_kv_m(IC(NOUT2), 31, KVD);
    put_o<0>(of, lds0 + ORING.b0, w4);
    wait_bar<8>();
    gl.w(0, ORING.w0); gl.w(1, ORING.w1); gl.w(2, ORING.w2);
    put_o<1>(of, lds0 + ORING.b1, w4);
    wait_bar<12>();
    f32x4 acc[7][4] = {};
    const Gemm<false> gm(lds0, w4);
    rawbar();
    STAMP(1, 0);
    wait_bar<0>();
    STAMP(1, 2);
    gm.run(acc, ORING);
    STAMP(1, 3);
#pragma unroll
    for (unsigned d = 0; d < 4; ++d)
      if (d != g) {
        const unsigned vo = (bid0 + 2 * d) * 65536 + g * 16384 + w4 * 4096 + lane * 16;
#pragma unroll
        for (int jj = 0; jj < 2; ++jj)
#pragma unroll
          for (int n = 0; n < 2; ++n)
            if (2 * d + jj < 7) {
              const f32x4 a = acc[2 * d + jj][2 * n], b = acc[2 * d + jj][2 * n + 1];
              const u32x4 w = {cvtpk(a[0], a[1]), cvtpk(a[2], a[3]), cvtpk(b[0], b[1]), cvtpk(b[2], b[3])};
              __builtin_amdgcn_raw_buffer_store_b128(w, srdE, vo + (jj * 2 + n) * 1024, 0, 16);
            }
      }
    f32x4 t[2][4];
#pragma unroll
    for (int n = 0; n < 4; ++n) {
      t[0][n] = g == 0 ? acc[0][n] : g == 1 ? acc[2][n] : g == 2 ? acc[4][n] : acc[6][n];
      t[1][n] = g == 0 ? acc[1][n] : g == 1 ? acc[3][n] : acc[5][n];
    }
    wait_vm<0>();
    STAMP(1, 4);
    if (lane < 4) st_flag(ws + WS_G + (((bid0 + 2 * lane) * 4 + w4) * 4 + g) * 64, tag);
    poll(ws + WS_G + ((bid * 4 + w4) * 4 + (lane & 3)) * 64, lane < 4, tag);
    u32x4 e[3][2][2];
#pragma unroll
    for (int jj = 0; jj < 2; ++jj)
#pragma unroll
      for (int n = 0; n < 2; ++n)
#pragma unroll
        for (unsigned sr = 0; sr < 3; ++sr)
          e[sr][jj][n] = __builtin_amdgcn_raw_buffer_load_b128(srdE, (jj && g == 3) ? 0x80000000u : lane * 16 + (jj * 2 + n) * 1024, bid * 65536 + ((g + sr + 1) & 3) * 16384 + w4 * 4096, 16);
#pragma unroll
    for (int jj = 0; jj < 2; ++jj) {
      u16* o = outp + (size_t)((2 * g + jj) * 16 + l15) * 2048 + l4 * 4;
#pragma unroll
      for (int n = 0; n < 4; ++n) {
        f32x4 v = t[jj][n];
#pragma unroll
        for (int sr = 0; sr < 3; ++sr) {
          const unsigned x = e[sr][jj][n >> 1][2 * (n & 1)], y = e[sr][jj][n >> 1][2 * (n & 1) + 1];
          v += f32x4{__uint_as_float(x << 16), __uint_as_float(x & 0xffff0000u), __uint_as_float(y << 16), __uint_as_float(y & 0xffff0000u)};
        }
        u32x2 w = {cvtpk(v[0], v[1]), cvtpk(v[2], v[3])};
        if (!(jj && g == 3)) *(GAS u32x2*)(o + n * 16) = w;
      }
    }
    if (g == 0 && hh == 0 && wid == 0 && lane == 0) st_flag(epoch, tag);
    STAMP(1, 5); STAMP(1, 6); STAMP(1, 7); STAMP(0, 3);
  } else {
    Attn at;
    {
      u32x4 rk[6][8];
      const rsrc_t srdK = mkrsrc(kptr, KVBYTES);
      auto fetch = [&](auto RI) {
        constexpr int r = decltype(RI)::value;
        if constexpr (r < NR) {
#pragma unroll
          for (int i = 0; i < 4; ++i) ld16nt(rk[r][4 + i], voffK, srdK, so_k(RT[r]), i * 4096);
#pragma unroll
          for (int i = 0; i < 4; ++i) ld16nt(rk[r][i], voffV, srdV, so_v(RT[r]), i * 4096);
        }
      };
      auto put = [&](auto RI, unsigned slot) {
        constexpr int r = decltype(RI)::value;
        if constexpr (r < NR) {
#pragma unroll
          for (int i = 0; i < 8; ++i) *(LAS u32x4*)(size_t)(lds0 + slot + w4 * 1024 + lane * 16 + i * 4096) = rk[r][i];
        }
      };
      const GemmLoad<true> gl(srdX, srdWq, lds0, w4, hh, p);
      auto qload = [&](auto PHASE) {
        constexpr int PH = decltype(PHASE)::value;
        Ring r = QRING;
        gl.tile(0, r.b0, r.w0); rawbar();
        gl.tile(1, r.b1, r.w1);
        wait_bar<11>();
        gl.tile(2, r.b2, r.w2);
#pragma unroll
        for (int v = 0; v < 13; ++v) {
          constexpr unsigned BM = BMASK << (2 + PH);
          const int nb = ((BM >> v) & 1) + ((BM >> (v + 1)) & 1);
          if (nb == 0) wait_bar<11>(); else if (nb == 1) wait_bar<19>(); else wait_bar<27>();
          gl.tile(v + 3, r.b0, r.w0);
          if (v == RB[0] + PH) fetch(IC(0)); if (v == RB[1] + PH) fetch(IC(1)); if (v == RB[2] + PH) fetch(IC(2));
          if (v == RB[3] + PH) fetch(IC(3)); if (v == RB[4] + PH) fetch(IC(4)); if (v == RB[5] + PH) fetch(IC(5));
          r.rotate();
        }
        issue_k(-1, KVD);
        if constexpr (((BMASK << PH) >> 12) & 1) wait_bar<23>(); else wait_bar<15>();
        issue_kv(0, KVA);
        wait_bar<12>();
#pragma unroll
        for (int r = 0; r < NR; ++r) landed(rk[r]);
      };
#define STAG 1
      if (hh & 1) qload(IC(STAG)); else qload(IC(0));
      rawbar();
      issue_kv(1, KVB);
      wait_bar<16>();
      STAMP(0, 1); CSTAMP(0, 5);
      wait_bar<8>();
      put(IC(0), KVC);
      unsigned ks = KVD, ks1 = KVA, ks2 = KVB, ks3 = KVC;
#pragma unroll
      for (int n = 0; n < 15; ++n) {
        const int u = n + 3;
        if ((RMASK >> u) & 1) {
          if (u == RT[1]) put(IC(1), ks); else if (u == RT[2]) put(IC(2), ks); else if (u == RT[3]) put(IC(3), ks);
          else if (u == RT[4]) put(IC(4), ks); else put(IC(5), ks);
        } else issue_kv_m(IC(NOUT1), u, ks);
        const unsigned t = ks; ks = ks1; ks1 = ks2; ks2 = ks3; ks3 = t;
        const int nd = 2 - ((RMASK >> (n + 2)) & 1) - ((RMASK >> u) & 1);
        if (nd == 2) wait_bar<16>(); else if (nd == 1) wait_bar<8>(); else wait_bar<0>();
      }
    }
    load_tag();
    at.init(lds0, w4, QI2, KVD, KVA);
    STAMP(0, 2); CSTAMP(0, 6);
    at.template run<0>();
    STAMP(1, 0);
    bar();
    u32x4 of[8];
    at.store(of, srdO, (32 + hh) * 28672, w4);
    put_o<0>(of, lds0 + ORING.b0 + 14336, w4); put_o<1>(of, lds0 + ORING.b1 + 14336, w4);
    const GemmLoad<false> gl(srdO, srdWo, lds0, w4, hh, p);
    bar();
    wait_vm<0>();
    if (lane == 0) st_flag(fam + 2048 + (hh * 4 + w4) * 64, tag);
    rawbar();
    Ring r = ORING;
    gl.w(3, r.w0);
    STAMP(1, 1);
    poll(fam + 2048 + (lane & 31) * 64, true, tag);
    STAMP(1, 2);
    gl.b(2, r.b2); gl.b(3, r.b0);
    wait_bar<7>();
    r.rotate();
    gl.tile(4, r.b0, r.w0);
    r.rotate();
#pragma unroll 1
    for (unsigned v = 2; v < 13; ++v) { wait_bar<11>(); gl.tile(v + 3, r.b0, r.w0); r.rotate(); }
    wait_bar<11>();
    wait_bar<0>();
    rawbar();
    STAMP(1, 3); STAMP(1, 4);
    STAMP(1, 5); STAMP(1, 6); STAMP(1, 7); STAMP(0, 3);
  }
}

extern "C" void kernel_launch(void* const* d_in, const int* in_sizes, int n_in, void* d_out, int out_size, void* d_ws, size_t ws_size, hipStream_t stream) {
  (void)hipFuncSetAttribute((const void*)k_fused, hipFuncAttributeMaxDynamicSharedMemorySize, 163840);
  k_fused<<<256, 512, 163840, stream>>>((const u16*)d_in[0], (const u16*)d_in[1], (const u16*)d_in[2], (const u16*)d_in[3], (const u16*)d_in[4],
                                        (const u16*)d_in[5], (const u16*)d_in[6], (const u16*)d_in[7], (const u16*)d_in[8], (const u16*)d_in[9],
                                        (char*)d_ws, (u16*)d_out);
}
```

```cpp
#include <hip/hip_runtime.h>
#include <stdint.h>
#include <type_traits>

typedef unsigned short u16;
typedef unsigned long long u64;
typedef __bf16 bf16x8 __attribute__((ext_vector_type(8)));
typedef __bf16 bf16x4 __attribute__((ext_vector_type(4)));
typedef __bf16 bf16x2 __attribute__((ext_vector_type(2)));
typedef float f32x16 __attribute__((ext_vector_type(16)));
typedef float f32x4 __attribute__((ext_vector_type(4)));
typedef float f32x2 __attribute__((ext_vector_type(2)));
typedef unsigned u32x4 __attribute__((ext_vector_type(4)));
typedef unsigned u32x2 __attribute__((ext_vector_type(2)));
#define LAS __attribute__((address_space(3)))
#define GAS __attribute__((address_space(1)))
#define DI __device__ __forceinline__
#define SB() __builtin_amdgcn_sched_barrier(0)
#define LGKM(n) do { __builtin_amdgcn_s_waitcnt(0xC07F | ((n) << 8)); SB(); } while (0)
#define IC(n) std::integral_constant<int, n>{}
typedef __amdgpu_buffer_rsrc_t rsrc_t;

constexpr int SQ = 112, DM = 1024, NH = 32, SK = 1024, DH = 128;
constexpr size_t WS_O = 0;
constexpr size_t WS_X = (size_t)16 << 20;
constexpr size_t WS_F = (size_t)48 << 20;
constexpr size_t WS_G = WS_F + 131072;
constexpr size_t WS_E = WS_G + 524288;
constexpr size_t WS_ST = (size_t)64 << 20;
constexpr float QSCALE = 0.08838834764831845f * 1.4426950408889634f;
constexpr float THR = 8.0f;
constexpr unsigned SPIN = 1u << 18;

#define NOUT1 24
#define NOUT2 24
#define MGR 2
#define SLP1 0
#define PB1 97
#define PA1 1650
#define PB2 113
#define PA2 3210
#define RDN 22
#define QW 8
#define NRT 3
#define RTILES {2, 7, 12, 0, 0, 0}
#define RBURST {3, 7, 10, 99, 99, 99}
#define CSTAMP(kern, idx) do {} while (0)
#define STAMP(kern, idx) do {} while (0)

DI rsrc_t mkrsrc(const void* p, unsigned bytes) { return __builtin_amdgcn_make_buffer_rsrc((void*)p, (short)0, (int)bytes, 0x00020000); }
template <int POL> DI void dma16(unsigned mb, unsigned mi, unsigned voff, rsrc_t srd, unsigned sb, unsigned si) {
  unsigned t;
  if constexpr (POL == 1)      asm volatile("s_add_u32 m0, %1, %2\n\ts_add_u32 %0, %5, %6\n\tbuffer_load_dwordx4 %3, %4, %0 offen nt lds"  : "=&s"(t) : "s"(mb), "n"(mi), "v"(voff), "s"(srd), "s"(sb), "n"(si) : "memory", "scc");
  else if constexpr (POL == 2) asm volatile("s_add_u32 m0, %1, %2\n\ts_add_u32 %0, %5, %6\n\tbuffer_load_dwordx4 %3, %4, %0 offen sc1 lds" : "=&s"(t) : "s"(mb), "n"(mi), "v"(voff), "s"(srd), "s"(sb), "n"(si) : "memory", "scc");
  else                         asm volatile("s_add_u32 m0, %1, %2\n\ts_add_u32 %0, %5, %6\n\tbuffer_load_dwordx4 %3, %4, %0 offen lds"     : "=&s"(t) : "s"(mb), "n"(mi), "v"(voff), "s"(srd), "s"(sb), "n"(si) : "memory", "scc");
}
DI void ld16nt(u32x4& d, unsigned voff, rsrc_t srd, unsigned sb, unsigned si) {
  unsigned t;
  asm volatile("s_add_u32 %1, %4, %5\n\tbuffer_load_dwordx4 %0, %2, %3, %1 offen nt" : "=v"(d), "=&s"(t) : "v"(voff), "s"(srd), "s"(sb), "n"(si) : "memory", "scc");
}
DI void landed(u32x4 (&r)[8]) { asm volatile("" : "+v"(r[0]), "+v"(r[1]), "+v"(r[2]), "+v"(r[3]), "+v"(r[4]), "+v"(r[5]), "+v"(r[6]), "+v"(r[7])); }
template <int N> DI void wait_bar() { asm volatile("s_waitcnt vmcnt(%0) lgkmcnt(0)\n\ts_barrier" ::"n"(N) : "memory"); }
template <int N> DI void wait_vm() { asm volatile("s_waitcnt vmcnt(%0)" ::"n"(N) : "memory"); }
DI void bar() { asm volatile("s_waitcnt lgkmcnt(0)\n\ts_barrier" ::: "memory"); }
DI void rawbar() { asm volatile("s_barrier" ::: "memory"); }
DI bf16x8 lds128(unsigned a) { return *(const LAS bf16x8*)(size_t)a; }
DI bf16x4 ldstr(unsigned a) { return __builtin_amdgcn_ds_read_tr16_b64_v4bf16((LAS bf16x4*)(size_t)a); }
DI bf16x8 cat(bf16x4 a, bf16x4 b) { return __builtin_shufflevector(a, b, 0, 1, 2, 3, 4, 5, 6, 7); }
DI f32x16 mfma32(bf16x8 a, bf16x8 b, f32x16 c) { return __builtin_amdgcn_mfma_f32_32x32x16_bf16(a, b, c, 0, 0, 0); }
DI f32x4 mfma16(bf16x8 a, bf16x8 b, f32x4 c) { return __builtin_amdgcn_mfma_f32_16x16x32_bf16(a, b, c, 0, 0, 0); }
DI void mac16(f32x4& c, bf16x8 a, bf16x8 b) { asm("v_mfma_f32_16x16x32_bf16 %0, %1, %2, %0" : "+v"(c) : "v"(a), "v"(b)); }
DI unsigned cvtpk(float lo, float hi) { f32x2 v = {lo, hi}; return __builtin_bit_cast(unsigned, __builtin_convertvector(v, bf16x2)); }
DI u32x4 pack8(const f32x16& a, int o, float s) {
  u32x4 w = {cvtpk(a[o] * s, a[o + 1] * s), cvtpk(a[o + 2] * s, a[o + 3] * s), cvtpk(a[o + 4] * s, a[o + 5] * s), cvtpk(a[o + 6] * s, a[o + 7] * s)};
  return w;
}
DI float fadd(float a, float b) { float r; asm("v_add_f32_e32 %0, %1, %2" : "=v"(r) : "v"(a), "v"(b)); return r; }
DI void vadd(unsigned& a, int d) { asm volatile("v_add_u32_e32 %0, %1, %0" : "+v"(a) : "s"(d)); }
DI float max2f(float a, float b) { return __builtin_elementwise_maximum(a, b); }
DI float max3f(float a, float b, float c) { return max2f(max2f(a, b), c); }
DI float xrow_max(float x) {
  auto a = __builtin_amdgcn_permlane16_swap(__float_as_uint(x), __float_as_uint(x), false, false);
  x = max2f(__uint_as_float(a[0]), __uint_as_float(a[1]));
  auto b = __builtin_amdgcn_permlane32_swap(__float_as_uint(x), __float_as_uint(x), false, false);
  return max2f(__uint_as_float(b[0]), __uint_as_float(b[1]));
}
DI float xhalf_max(float x) { auto r = __builtin_amdgcn_permlane32_swap(__float_as_uint(x), __float_as_uint(x), false, false); return max2f(__uint_as_float(r[0]), __uint_as_float(r[1])); }
DI float xhalf_sum(float x) { auto r = __builtin_amdgcn_permlane32_swap(__float_as_uint(x), __float_as_uint(x), false, false); return __uint_as_float(r[0]) + __uint_as_float(r[1]); }
DI void st_flag(char* p, u64 v) { __hip_atomic_store((GAS u64*)p, v, __ATOMIC_RELAXED, __HIP_MEMORY_SCOPE_AGENT); }
DI u64 ld_flag(const char* p) { return __hip_atomic_load((GAS u64*)p, __ATOMIC_RELAXED, __HIP_MEMORY_SCOPE_AGENT); }
DI void poll(const char* p, bool on, u64 tag) {
#pragma unroll 1
  for (unsigned spin = 0; spin < SPIN; ++spin) {
    const u64 f = on ? ld_flag(p) : tag;
    if (__all(f == tag)) break;
    __builtin_amdgcn_s_sleep(1);
  }
  asm volatile("" ::: "memory");
}

constexpr unsigned KVA = 0, KVB = 65536, KVC = 98304, KVD = 32768;
constexpr unsigned QI1 = 98304, QI2 = 131072;
struct Ring { unsigned b0, b1, b2, w0, w1, w2; DI void rotate() { const unsigned t = b0, u = w0; b0 = b1; b1 = b2; b2 = t; w0 = w1; w1 = w2; w2 = u; } };
constexpr Ring QRING = {118784, 0, 73728, 118784 + 28672, 28672, 73728 + 28672};
constexpr Ring ORING = {16384, 73728, 45056, 131072, 147456, 0};

template <bool ISQ> struct Gemm {
  struct Frag { bf16x8 a[4], b[7]; };
  unsigned abase, bbase;
  DI Gemm(unsigned lds0, unsigned cw) {
    const unsigned lane = threadIdx.x & 63, l4 = lane >> 4, l15 = lane & 15, q4 = (lane >> 2) & 3, pp = lane & 3;
    abase = lds0 + (ISQ ? 8 * l4 + q4 : 4 * l4 + q4) * 256 + (((4 * (cw & 1)) ^ (ISQ ? (q4 << 1) | (l4 & 1) : 4 * (l4 & 1) + q4)) << 5) + pp * 8;
    bbase = lds0 + (cw >> 1) * 14336 + (ISQ ? l15 * 128 + ((l4 ^ (l15 >> 1)) << 4) : lane * 16);
  }
  struct Half { bf16x4 a[8]; };
  DI void rd(Frag& f, Half& h, int i, unsigned bs, unsigned ws, int s) const {
    if (i < 8) { h.a[i] = ldstr(((abase + ws) ^ ((i >> 1) << 5)) + s * 8192 + (i & 1) * (ISQ ? 1024 : 4096)); if (i & 1) f.a[i >> 1] = cat(h.a[i - 1], h.a[i]); }
    else if (i < 15) { const int j = i - 8; f.b[j] = ISQ ? lds128(((bbase + bs) ^ (s << 6)) + j * 2048) : lds128(bbase + bs + s * 7168 + j * 1024); }
  }
  template <bool RD> DI void half(f32x4 (&acc)[7][4], const Frag& c, Frag& n, unsigned bs, unsigned ws, int s) const {
    Half h;
#pragma unroll
    for (int m = 0; m < 28; ++m) {
      acc[m >> 2][m & 3] = mfma16(c.a[m & 3], c.b[m >> 2], acc[m >> 2][m & 3]);
      SB();
      if constexpr (RD) { if (m < RDN) { if (RDN == 15) rd(n, h, m, bs, ws, s); else if ((m % 3) != 2) rd(n, h, m - m / 3, bs, ws, s); } SB(); }
    }
  }
  DI void run(f32x4 (&acc)[7][4], Ring r) const {
    Frag f0, f1;
    {
      Half h;
#pragma unroll
      for (int i = 0; i < 15; ++i) rd(f0, h, i, r.b0, r.w0, 0);
    }
#pragma unroll 1
    for (unsigned v = 0; v < 15; ++v) {
      LGKM(0);
      half<true>(acc, f0, f1, r.b0, r.w0, 1);
      LGKM(0); rawbar(); SB();
      half<true>(acc, f1, f0, r.b1, r.w1, 0);
      r.rotate();
    }
    LGKM(0);
    half<true>(acc, f0, f1, r.b0, r.w0, 1);
    LGKM(0); rawbar(); SB();
    half<false>(acc, f1, f0, 0, 0, 0);
  }
};
template <bool ISQ> struct GemmLoad {
  static constexpr unsigned WROW = ISQ ? 8192 : 2048;
  rsrc_t srdB, srdW;
  unsigned voffW, vB0, lds0, lw, hh, p;
  DI GemmLoad(rsrc_t b, rsrc_t w, unsigned lds0_, unsigned lw_, unsigned hh_, unsigned p_) : srdB(b), srdW(w), lds0(lds0_), lw(lw_), hh(hh_), p(p_) {
    const unsigned lane = threadIdx.x & 63, l4 = lane >> 4, l15 = lane & 15;
    voffW = l4 * WROW + ((l15 ^ (ISQ ? (l4 << 2) | (lw & 2) : (((lw & 1) << 2) | l4) << 1)) << 4);
    vB0 = ISQ ? (lane >> 3) * 2048 + ((((lane & 7) ^ l4) << 4) ^ ((lw & 1) << 6)) : lane * 16;
  }
  DI unsigned kk(unsigned v) const {
    if (ISQ) return (v & 8) | ((v + hh + 2 * p) & 7);
    unsigned j = v + ((7 * p) >> 1);
    j = j >= 16 ? j - 14 : j;
    return (2 * hh + (v < 2 ? v : j)) & 15;
  }
  DI void w(unsigned v, unsigned slot) const {
#pragma unroll
    for (unsigned i = 0; i < 4; ++i) dma16<0>(lds0 + slot + lw * 1024, i * 4096, voffW, srdW, (kk(v) * 64 + 4 * lw) * WROW, 16 * i * WROW);
  }
  DI void b(unsigned v, unsigned slot) const {
    if constexpr (ISQ) {
#pragma unroll
      for (unsigned i = 0; i < 7; ++i) dma16<0>(lds0 + slot + lw * 1024, i * 4096, vB0, srdB, lw * 16384 + kk(v) * 128, i * 65536);
    } else {
#pragma unroll
      for (unsigned i = 0; i < 3; ++i) dma16<2>(lds0 + slot + lw * 1024, i * 4096, vB0, srdB, kk(v) * 14336 + lw * 1024, i * 4096);
      const unsigned x = lw < 2 ? 12288 + lw * 1024 : 14336 + 12288 + (3 - lw) * 1024;
      dma16<2>(lds0 + slot + x, 0, vB0, srdB, kk(v) * 14336 + x + (lw < 2 ? 0 : 32 * 28672 - 14336), 0);
#pragma unroll
      for (unsigned i = 0; i < 3; ++i) dma16<2>(lds0 + slot + (3 - lw) * 1024, 14336 + i * 4096, vB0, srdB, kk(v) * 14336 + (3 - lw) * 1024, 32 * 28672 + i * 4096);
    }
  }
  DI void tile(unsigned v, unsigned bs, unsigned ws) const { w(v, ws); b(v, bs); }
};

struct Score { f32x4 s[2][2]; };
struct Attn {
  f32x4 o[8][2], ls[2], negm[2];
  Score sa, sb;
  bf16x8 qf[4][2], kf[8], vf[8], ones;
  bf16x4 vh;
  float mref[2];
  unsigned ka[4], va[8];
  DI void rd_k(int f, int ofs) { kf[f] = lds128(ka[f >> 1] + ofs + (f & 1) * 4096); }
  DI void rd_v(int i, int ofs) { const bf16x4 x = ldstr(va[i >> 1] + ofs + (i & 1) * 4096); if (i & 1) vf[i >> 1] = cat(vh, x); else vh = x; }
  DI void qk(Score& n, int i) {
    const int f = i >> 1, b = i & 1;
    if (f < 2) asm("v_mfma_f32_16x16x32_bf16 %0, %1, %2, %3" : "=&v"(n.s[f][b]) : "v"(kf[f]), "v"(qf[0][b]), "v"(negm[b]));
    else mac16(n.s[f & 1][b], kf[f], qf[f >> 1][b]);
    if (i == 7) asm volatile("" :: "v"(negm[0]), "v"(negm[1]));
  }
  DI void settle() {
    asm volatile("s_nop 7" : "+v"(o[0][0]), "+v"(o[0][1]), "+v"(o[1][0]), "+v"(o[1][1]), "+v"(o[2][0]), "+v"(o[2][1]), "+v"(o[3][0]), "+v"(o[3][1]), "+v"(o[4][0]), "+v"(o[4][1]),
                 "+v"(o[5][0]), "+v"(o[5][1]), "+v"(o[6][0]), "+v"(o[6][1]), "+v"(o[7][0]), "+v"(o[7][1]), "+v"(ls[0]), "+v"(ls[1]));
  }
  template <bool FIRST> DI void rescale(Score& n, bool grow, bool pad) {
    if (FIRST || __builtin_expect(grow, 0)) {
      if constexpr (!FIRST) settle();
#pragma unroll
      for (int b = 0; b < 2; ++b) {
        const f32x4 &x = n.s[0][b], &y = n.s[1][b];
        const float mx = xrow_max(max2f(max3f(max3f(x[0], x[1], x[2]), x[3], y[0]), max3f(y[1], y[2], y[3])));
        const float d = FIRST ? ((b && pad) ? 1e30f : mx) : max2f(mx, 0.f);
        mref[b] += d;
        n.s[0][b] -= d; n.s[1][b] -= d;
        negm[b] = f32x4{-mref[b], -mref[b], -mref[b], -mref[b]};
        if constexpr (!FIRST) {
          const float f = __builtin_amdgcn_exp2f(-d);
          ls[b] *= f;
#pragma unroll
          for (int c = 0; c < 8; ++c) o[c][b] *= f;
        }
      }
      asm volatile("s_nop 1" : "+v"(negm[0]), "+v"(negm[1]));
    }
  }
  DI void advance(int delta) {
#pragma unroll
    for (int i = 0; i < 4; ++i) ka[i] += delta;
#pragma unroll
    for (int i = 0; i < 8; ++i) va[i] += delta;
  }
  DI void init(unsigned lds0, unsigned w4, unsigned qimg, unsigned kslot, unsigned slot0) {
    const unsigned lane = threadIdx.x & 63, l15 = lane & 15, l4 = lane >> 4, q4 = (lane >> 2) & 3, pp = lane & 3;
    const unsigned kb = l15 * 256 + ((l4 ^ l15) << 4);
    const unsigned vb = (4 * l4 + q4) * 256 + ((4 * (l4 & 1) + q4) << 5) + pp * 8;
    const bool pad = w4 == 3;
#pragma unroll
    for (int i = 0; i < 4; ++i) {
      ka[i] = lds0 + kslot + 16384 + (kb ^ (i << 6));
      const unsigned a = lds0 + qimg + w4 * 8192 + (kb ^ (i << 6));
      qf[i][0] = lds128(a); qf[i][1] = lds128(a + (pad ? 0 : 4096));
      rd_k(2 * i, 0); rd_k(2 * i + 1, 0);
      SB();
    }
#pragma unroll
    for (int c = 0; c < 8; ++c) { o[c][0] = f32x4{}; o[c][1] = f32x4{}; va[c] = lds0 + kslot + (vb ^ (c << 5)); }
    if (pad) {
#pragma unroll
      for (int i = 0; i < 4; ++i) qf[i][1] = bf16x8{};
    }
    ones = __builtin_bit_cast(bf16x8, u32x4{0x3f803f80u, 0x3f803f80u, 0x3f803f80u, 0x3f803f80u});
    ls[0] = ls[1] = negm[0] = negm[1] = f32x4{}; mref[0] = mref[1] = 0.f;
    SB();
    asm volatile("s_nop 1" : "+v"(negm[0]), "+v"(negm[1]), "+v"(ones), "+v"(qf[0][1]));
#pragma unroll
    for (int i = 0; i < 16; ++i) { qk(sa, i); SB(); }
#pragma unroll
    for (int f = 0; f < 8; ++f) rd_k(f, 8192);
    asm volatile("s_nop 7" : "+v"(sa.s[0][0]), "+v"(sa.s[0][1]), "+v"(sa.s[1][0]), "+v"(sa.s[1][1]));
    rescale<true>(sa, true, pad);
    advance(slot0 - kslot);
    SB();
  }
  template <bool NEXT, bool RDK, bool ADV> DI void sub(Score& c, Score& n, int ofs, int delta) {
    u32x4 p[2];
    LGKM(0);
#pragma unroll
    for (int i = 0; i < 16; ++i) {
      if constexpr (NEXT) { qk(n, i); SB(); }
      if (i & 1) { rd_v(i >> 1, ofs); SB(); }
      f32x4& x = c.s[(i >> 2) & 1][i >> 3];
      x[i & 3] = __builtin_amdgcn_exp2f(x[i & 3]);
      if (i >= 2 && !(i & 1)) { const int k = i - 2; const f32x4& y = c.s[(k >> 2) & 1][k >> 3]; p[k >> 3][(k >> 1) & 3] = cvtpk(y[k & 3], y[(k & 3) + 1]); }
      SB();
    }
    float m0, m1;
    bool grow = false;
#pragma unroll
    for (int i = 0; i < 18; ++i) {
      if (i == 0) { LGKM(4); p[1][3] = cvtpk(c.s[1][1][2], c.s[1][1][3]); SB(); }
      if (i == 4) LGKM(RDK ? 5 : 4);
      if (RDK && i == 8) LGKM(8);
      if (RDK && i == 12) LGKM(4);
      if (i < 16) mac16(o[i >> 1][i & 1], vf[i >> 1], __builtin_bit_cast(bf16x8, p[i & 1]));
      else mac16(ls[i & 1], ones, __builtin_bit_cast(bf16x8, p[i & 1]));
      SB();
      if (i < 8) rd_v(8 + i, ofs);
      if constexpr (RDK) { if (i >= 3 && i < 11) rd_k(i - 3, ofs); }
      if constexpr (NEXT) {
        if (i == 1) m0 = max3f(n.s[0][0][0], n.s[0][0][1], n.s[0][0][2]);
        if (i == 2) m1 = max3f(n.s[0][0][3], n.s[0][1][0], n.s[0][1][1]);
        if (i == 8) m0 = max3f(m0, n.s[0][1][2], n.s[0][1][3]);
        if (i == 9) m1 = max3f(m1, n.s[1][0][0], n.s[1][0][1]);
        if (i == 10) m0 = max3f(m0, n.s[1][0][2], n.s[1][0][3]);
        if (i == 11) { m1 = max3f(m1, n.s[1][1][0], n.s[1][1][1]); m0 = max3f(m0, n.s[1][1][2], n.s[1][1][3]); }
        if (i == 12) m0 = max2f(m0, m1);
        if (i == 13) grow = __any(m0 > THR);
      }
      if constexpr (ADV) {
        if (i >= 12 && i < 16) { vadd(va[2 * i - 24], delta); vadd(va[2 * i - 23], delta); }
        if (i >= 16) { vadd(ka[2 * i - 32], delta); vadd(ka[2 * i - 31], delta); }
      }
      SB();
    }
    if constexpr (NEXT) rescale<false>(n, grow, false);
  }
  template <int SLP> DI void run() {
    unsigned s0 = KVA, s1 = KVB, s2 = KVC, s3 = KVD;
    wait_bar<8>();
#pragma unroll 1
    for (unsigned n = 0; n < 15; ++n) {
      sub<true, true, false>(sa, sb, 0, 0);
      sub<true, true, true>(sb, sa, 8192, s1 - s0);
      if (SLP) __builtin_amdgcn_s_sleep(SLP);
      const unsigned t = s0; s0 = s1; s1 = s2; s2 = s3; s3 = t;
      wait_bar<0>();
    }
    sub<true, false, false>(sa, sb, 0, 0);
    sub<false, false, false>(sb, sa, 8192, 0);
    settle();
  }
  DI void finish(u32x4 (&of)[8]) {
#pragma unroll
    for (int b = 0; b < 2; ++b) {
      const float inv = 1.f / ls[b][0];
#pragma unroll
      for (int s = 0; s < 4; ++s) {
        const f32x4 x = o[2 * s][b] * inv, y = o[2 * s + 1][b] * inv;
        of[2 * s + b] = u32x4{cvtpk(x[0], x[1]), cvtpk(x[2], x[3]), cvtpk(y[0], y[1]), cvtpk(y[2], y[3])};
      }
    }
  }
};
DI void publish_o(const u32x4 (&of)[8], rsrc_t srdO, unsigned so, unsigned w4) {
  const unsigned vo = so + 2 * w4 * 1024 + (threadIdx.x & 63) * 16, vo1 = w4 == 3 ? 0x80000000u : vo + 1024;
#pragma unroll
  for (int s = 0; s < 4; ++s) {
    __builtin_amdgcn_raw_buffer_store_b128(of[2 * s], srdO, vo + s * 7168, 0, 16);
    __builtin_amdgcn_raw_buffer_store_b128(of[2 * s + 1], srdO, vo1 + s * 7168, 0, 16);
  }
}
template <int T> DI void put_o(const u32x4 (&of)[8], unsigned base, unsigned w4) {
  const unsigned a = base + 2 * w4 * 1024 + (threadIdx.x & 63) * 16;
  *(LAS u32x4*)(size_t)a = of[4 * T]; *(LAS u32x4*)(size_t)(a + 7168) = of[4 * T + 2];
  if (w4 < 3) { *(LAS u32x4*)(size_t)(a + 1024) = of[4 * T + 1]; *(LAS u32x4*)(size_t)(a + 8192) = of[4 * T + 3]; }
}

DI void write_q(const f32x4 (&acc)[7][4], unsigned base, unsigned dh) {
  const unsigned lane = threadIdx.x & 63, l4 = lane >> 4, l15 = lane & 15;
#pragma unroll
  for (int n = 0; n < 4; ++n) {
    const unsigned a = base + l15 * 256 + (l4 & 1) * 8 + (((8 * dh + 2 * n + (l4 >> 1)) ^ l15) << 4);
#pragma unroll
    for (int j = 0; j < 7; ++j) {
      u32x2 w = {cvtpk(acc[j][n][0] * QSCALE, acc[j][n][1] * QSCALE), cvtpk(acc[j][n][2] * QSCALE, acc[j][n][3] * QSCALE)};
      *(LAS u32x2*)(size_t)(a + j * 4096) = w;
    }
  }
}

constexpr int NR = NRT;
constexpr int RT[6] = RTILES;
constexpr int RB[6] = RBURST;
constexpr unsigned mask_of(const int (&a)[6]) { unsigned m = 0; for (int i = 0; i < NR; ++i) m |= 1u << a[i]; return m; }
constexpr unsigned RMASK = mask_of(RT), BMASK = mask_of(RB);

__global__ __launch_bounds__(512, 2)
void k_fused(const u16* __restrict__ xA, const u16* __restrict__ kA, const u16* __restrict__ vA, const u16* __restrict__ wqA, const u16* __restrict__ woA,
             const u16* __restrict__ xB, const u16* __restrict__ kB, const u16* __restrict__ vB, const u16* __restrict__ wqB, const u16* __restrict__ woB,
             char* __restrict__ ws, u16* __restrict__ out) {
  extern __shared__ __attribute__((aligned(16))) char smem[];
  const unsigned lds0 = (unsigned)(size_t)(LAS char*)smem;
  const unsigned lane = threadIdx.x & 63, wid = __builtin_amdgcn_readfirstlane(threadIdx.x >> 6), w4 = wid & 3;
  const unsigned bid = blockIdx.x, xcd = bid & 7, rr = bid >> 3, br = xcd & 1, g = xcd >> 1, hh = rr & 7, p = rr >> 3;
  const unsigned head = 8 * g + hh;
#define KROT 0
  const unsigned krot = KROT;
  STAMP(0, 0); CSTAMP(0, 4);
  const unsigned t0 = (unsigned)wall_clock64();
  auto not_before = [&](int ticks) { while ((int)((unsigned)wall_clock64() - t0) < ticks) __builtin_amdgcn_s_sleep(1); };
  const char* const kptr = (const char*)(br ? kB : kA) + (size_t)(2 * p * NH + head) * (SK * DH * 2);
  const char* const vptr = (const char*)(br ? vB : vA) + (size_t)(2 * p * NH + head) * (SK * DH * 2);
  const rsrc_t srdX = mkrsrc((const char*)(br ? xB : xA) + (size_t)p * (2 * SQ * DM * 2), 2 * SQ * DM * 2);
  const rsrc_t srdWq = mkrsrc((const char*)(br ? wqB : wqA) + head * 256, DM * NH * DH * 2 - head * 256);
  const rsrc_t srdWo = mkrsrc((const char*)(br ? woB : woA) + (size_t)g * (1024 * 2048) + hh * 256, 1024 * 2048 - hh * 256);
  const rsrc_t srdO = mkrsrc(ws + WS_O + (size_t)((br * 8 + 2 * p) * 32 + 8 * g) * 28672, (32 + 8) * 28672);
  const rsrc_t srdE = mkrsrc(ws + WS_X, 256 * 65536);
  char* const epoch = ws + WS_E + (br * 4 + p) * 64;
  char* const fam = ws + WS_F + ((br * 4 + g) * 8 + 2 * p) * 2048;
  u16* const outp = out + (size_t)((2 * p + (w4 >> 1)) * SQ) * 2048 + br * 1024 + hh * 128 + (w4 & 1) * 64;
  asm volatile("" :: "s"(kptr), "s"(vptr), "s"(srdX), "s"(srdWq), "s"(srdWo), "s"(srdO), "s"(srdE), "s"(outp));
  u64 tag;
  auto load_tag = [&]() { asm volatile("s_load_dwordx2 %0, %1, 0x0\n\ts_waitcnt lgkmcnt(0)" : "=&s"(tag) : "s"(epoch) : "memory"); tag += 1; };
  const unsigned l4 = lane >> 4, l15 = lane & 15;
  const unsigned voffV = l4 * 256 + ((l15 ^ ((((w4 & 1) << 2) | l4) << 1)) << 4), voffK = l4 * 256 + ((l15 ^ ((w4 << 2) | l4)) << 4);
  constexpr unsigned KVBYTES = NH * SK * DH * 2 + SK * DH * 2;
  const rsrc_t srdV = mkrsrc(vptr, KVBYTES);
  auto so_k = [&](int n) { return ((n + 1) >> 4) * (NH * SK * DH * 2) + ((n + 1 + krot) & 15) * 16384 + w4 * 1024; };
  auto so_v = [&](int n) { return (n >> 4) * (NH * SK * DH * 2) + ((n + krot) & 15) * 16384 + w4 * 1024; };
  auto issue_k = [&](int n, unsigned slot) {
    const rsrc_t r = mkrsrc(kptr, n < 31 ? KVBYTES : 0);
#pragma unroll
    for (unsigned i = 0; i < 4; ++i) dma16<1>(lds0 + slot + w4 * 1024, 16384 + i * 4096, voffK, r, so_k(n), i * 4096);
  };
  auto issue_kv = [&](int n, unsigned slot) {
    issue_k(n, slot);
#pragma unroll
    for (unsigned i = 0; i < 4; ++i) dma16<1>(lds0 + slot + w4 * 1024, i * 4096, voffV, srdV, so_v(n), i * 4096);
  };
  auto issue_kv_m = [&](auto NO, int n, unsigned slot) {
    constexpr int N = decltype(NO)::value;
    const rsrc_t r = mkrsrc(kptr, n < 31 ? KVBYTES : 0);
#pragma unroll
    for (unsigned i = 0; i < 4; ++i) { if (i % MGR == 0) wait_vm<N - MGR>(); dma16<1>(lds0 + slot + w4 * 1024, 16384 + i * 4096, voffK, r, so_k(n), i * 4096); }
#pragma unroll
    for (unsigned i = 0; i < 4; ++i) { if (i % MGR == 0) wait_vm<N - MGR>(); dma16<1>(lds0 + slot + w4 * 1024, i * 4096, voffV, srdV, so_v(n), i * 4096); }
  };
  const unsigned bid0 = bid & ~6u;

  if (wid < 4) {
    Attn at;
    load_tag();
    {
      f32x4 acc[7][4] = {};
      const Gemm<true> gm(lds0, w4);
      rawbar(); rawbar();
      gm.run(acc, QRING);
      write_q(acc, lds0 + ((w4 >> 1) ? QI2 : QI1), w4 & 1);
    }
    bar();
    STAMP(0, 1); CSTAMP(0, 5);
    at.init(lds0, w4, QI1, KVD, KVA);
    at.template run<SLP1>();
    STAMP(0, 2); CSTAMP(0, 6);
    const GemmLoad<false> gl(srdO, srdWo, lds0, w4, hh, p);
    bar();
    issue_kv(18, KVC);
    u32x4 of[8];
    at.finish(of);
    {
      unsigned ks = KVD, ks1 = KVA, ks2 = KVB, ks3 = KVC;
#pragma unroll 1
      for (unsigned n = 17; n < 30; ++n) {
        wait_bar<8>();
        if (PB2) not_before(PA2 + (n - 16) * PB2);
        issue_kv_m(IC(NOUT2), n + 2, ks);
        const unsigned t = ks; ks = ks1; ks1 = ks2; ks2 = ks3; ks3 = t;
      }
    }
    publish_o(of, srdO, hh * 28672, w4);
    put_o<0>(of, lds0 + ORING.b0, w4);
    wait_bar<16>();
    gl.w(0, ORING.w0); gl.w(1, ORING.w1); gl.w(2, ORING.w2);
    put_o<1>(of, lds0 + ORING.b1, w4);
    wait_bar<20>();
    wait_vm<12>();
    if (lane == 0) st_flag(fam + (hh * 4 + w4) * 64, tag);
    f32x4 acc[7][4] = {};
    const Gemm<false> gm(lds0, w4);
    rawbar();
    STAMP(1, 0);
    wait_bar<0>();
    STAMP(1, 2);
    gm.run(acc, ORING);
    STAMP(1, 3);
#pragma unroll
    for (unsigned d = 0; d < 4; ++d)
      if (d != g) {
        const unsigned vo = (bid0 + 2 * d) * 65536 + g * 16384 + w4 * 4096 + lane * 16;
#pragma unroll
        for (int jj = 0; jj < 2; ++jj)
#pragma unroll
          for (int n = 0; n < 2; ++n)
            if (2 * d + jj < 7) {
              const f32x4 a = acc[2 * d + jj][2 * n], b = acc[2 * d + jj][2 * n + 1];
              const u32x4 w = {cvtpk(a[0], a[1]), cvtpk(a[2], a[3]), cvtpk(b[0], b[1]), cvtpk(b[2], b[3])};
              __builtin_amdgcn_raw_buffer_store_b128(w, srdE, vo + (jj * 2 + n) * 1024, 0, 16);
            }
      }
    f32x4 t[2][4];
#pragma unroll
    for (int n = 0; n < 4; ++n) {
      t[0][n] = g == 0 ? acc[0][n] : g == 1 ? acc[2][n] : g == 2 ? acc[4][n] : acc[6][n];
      t[1][n] = g == 0 ? acc[1][n] : g == 1 ? acc[3][n] : acc[5][n];
    }
    wait_vm<0>();
    STAMP(1, 4);
    if (lane < 4) st_flag(ws + WS_G + (((bid0 + 2 * lane) * 4 + w4) * 4 + g) * 64, tag);
    poll(ws + WS_G + ((bid * 4 + w4) * 4 + (lane & 3)) * 64, lane < 4, tag);
    u32x4 e[3][2][2];
#pragma unroll
    for (int jj = 0; jj < 2; ++jj)
#pragma unroll
      for (int n = 0; n < 2; ++n)
#pragma unroll
        for (unsigned sr = 0; sr < 3; ++sr)
          e[sr][jj][n] = __builtin_amdgcn_raw_buffer_load_b128(srdE, (jj && g == 3) ? 0x80000000u : lane * 16 + (jj * 2 + n) * 1024, bid * 65536 + ((g + sr + 1) & 3) * 16384 + w4 * 4096, 16);
#pragma unroll
    for (int jj = 0; jj < 2; ++jj) {
      u16* o = outp + (size_t)((2 * g + jj) * 16 + l15) * 2048 + l4 * 4;
#pragma unroll
      for (int n = 0; n < 4; ++n) {
        f32x4 v = t[jj][n];
#pragma unroll
        for (int sr = 0; sr < 3; ++sr) {
          const unsigned x = e[sr][jj][n >> 1][2 * (n & 1)], y = e[sr][jj][n >> 1][2 * (n & 1) + 1];
          v += f32x4{__uint_as_float(x << 16), __uint_as_float(x & 0xffff0000u), __uint_as_float(y << 16), __uint_as_float(y & 0xffff0000u)};
        }
        u32x2 w = {cvtpk(v[0], v[1]), cvtpk(v[2], v[3])};
        if (!(jj && g == 3)) *(GAS u32x2*)(o + n * 16) = w;
      }
    }
    if (g == 0 && hh == 0 && wid == 0 && lane == 0) st_flag(epoch, tag);
    STAMP(1, 5); STAMP(1, 6); STAMP(1, 7); STAMP(0, 3);
  } else {
    Attn at;
    {
      u32x4 rk[6][8];
      const rsrc_t srdK = mkrsrc(kptr, KVBYTES);
      auto fetch = [&](auto RI) {
        constexpr int r = decltype(RI)::value;
        if constexpr (r < NR) {
#pragma unroll
          for (int i = 0; i < 4; ++i) ld16nt(rk[r][4 + i], voffK, srdK, so_k(RT[r]), i * 4096);
#pragma unroll
          for (int i = 0; i < 4; ++i) ld16nt(rk[r][i], voffV, srdV, so_v(RT[r]), i * 4096);
        }
      };
      auto put = [&](auto RI, unsigned slot) {
        constexpr int r = decltype(RI)::value;
        if constexpr (r < NR) {
#pragma unroll
          for (int i = 0; i < 8; ++i) *(LAS u32x4*)(size_t)(lds0 + slot + w4 * 1024 + lane * 16 + i * 4096) = rk[r][i];
        }
      };
      const GemmLoad<true> gl(srdX, srdWq, lds0, w4, hh, p);
      auto qload = [&](auto PHASE) {
        constexpr int PH = decltype(PHASE)::value;
        Ring r = QRING;
        gl.tile(0, r.b0, r.w0); rawbar();
        gl.tile(1, r.b1, r.w1);
        wait_bar<11>();
        gl.tile(2, r.b2, r.w2);
#pragma unroll
        for (int v = 0; v < 13; ++v) {
          constexpr unsigned BM = BMASK << (2 + PH);
          const int nb = ((BM >> v) & 1) + ((BM >> (v + 1)) & 1);
          if (nb == 0) wait_bar<11>(); else if (nb == 1) wait_bar<19>(); else wait_bar<27>();
          gl.tile(v + 3, r.b0, r.w0);
          if (v == RB[0] + PH) fetch(IC(0)); if (v == RB[1] + PH) fetch(IC(1)); if (v == RB[2] + PH) fetch(IC(2));
          if (v == RB[3] + PH) fetch(IC(3)); if (v == RB[4] + PH) fetch(IC(4)); if (v == RB[5] + PH) fetch(IC(5));
          r.rotate();
        }
        issue_k(-1, KVD);
        if constexpr (((BMASK << PH) >> 12) & 1) wait_bar<23>(); else wait_bar<15>();
        issue_kv(0, KVA);
        wait_bar<12>();
#pragma unroll
        for (int r = 0; r < NR; ++r) landed(rk[r]);
      };
#define STAG 1
      if (hh & 1) qload(IC(STAG)); else qload(IC(0));
      rawbar();
      issue_kv(1, KVB);
      wait_bar<16>();
      STAMP(0, 1); CSTAMP(0, 5);
      wait_bar<8>();
      put(IC(0), KVC);
      unsigned ks = KVD, ks1 = KVA, ks2 = KVB, ks3 = KVC;
#pragma unroll
      for (int n = 0; n < 15; ++n) {
        const int u = n + 3;
        if ((RMASK >> u) & 1) {
          if (u == RT[1]) put(IC(1), ks); else if (u == RT[2]) put(IC(2), ks); else if (u == RT[3]) put(IC(3), ks);
          else if (u == RT[4]) put(IC(4), ks); else put(IC(5), ks);
        } else issue_kv_m(IC(NOUT1), u, ks);
        const unsigned t = ks; ks = ks1; ks1 = ks2; ks2 = ks3; ks3 = t;
        const int nd = 2 - ((RMASK >> (n + 2)) & 1) - ((RMASK >> u) & 1);
        if (PB1) not_before(PA1 + (n + 1) * PB1);
        if (nd == 2) wait_bar<16>(); else if (nd == 1) wait_bar<8>(); else wait_bar<0>();
      }
    }
    load_tag();
    at.init(lds0, w4, QI2, KVD, KVA);
    STAMP(0, 2); CSTAMP(0, 6);
    at.template run<0>();
    STAMP(1, 0);
    bar();
    u32x4 of[8];
    at.finish(of);
    publish_o(of, srdO, (32 + hh) * 28672, w4);
    put_o<0>(of, lds0 + ORING.b0 + 14336, w4); put_o<1>(of, lds0 + ORING.b1 + 14336, w4);
    const GemmLoad<false> gl(srdO, srdWo, lds0, w4, hh, p);
    bar();
    wait_vm<0>();
    if (lane == 0) st_flag(fam + 2048 + (hh * 4 + w4) * 64, tag);
    rawbar();
    Ring r = ORING;
    gl.w(3, r.w0);
    STAMP(1, 1);
    poll(fam + lane * 64, true, tag);
    STAMP(1, 2);
    gl.b(2, r.b2); gl.b(3, r.b0);
    wait_bar<7>();
    r.rotate();
    gl.tile(4, r.b0, r.w0);
    r.rotate();
#pragma unroll 1
    for (unsigned v = 2; v < 13; ++v) { wait_bar<11>(); gl.tile(v + 3, r.b0, r.w0); r.rotate(); }
    wait_bar<11>();
    wait_bar<0>();
    rawbar();
    STAMP(1, 3); STAMP(1, 4);
    STAMP(1, 5); STAMP(1, 6); STAMP(1, 7); STAMP(0, 3);
  }
}

extern "C" void kernel_launch(void* const* d_in, const int* in_sizes, int n_in, void* d_out, int out_size, void* d_ws, size_t ws_size, hipStream_t stream) {
  (void)hipFuncSetAttribute((const void*)k_fused, hipFuncAttributeMaxDynamicSharedMemorySize, 163840);
  k_fused<<<256, 512, 163840, stream>>>((const u16*)d_in[0], (const u16*)d_in[1], (const u16*)d_in[2], (const u16*)d_in[3], (const u16*)d_in[4],
                                        (const u16*)d_in[5], (const u16*)d_in[6], (const u16*)d_in[7], (const u16*)d_in[8], (const u16*)d_in[9],
                                        (char*)d_ws, (u16*)d_out);
}
```

```cpp
#include <hip/hip_runtime.h>
#include <stdint.h>
#include <type_traits>

typedef unsigned short u16;
typedef unsigned long long u64;
typedef __bf16 bf16x8 __attribute__((ext_vector_type(8)));
typedef __bf16 bf16x4 __attribute__((ext_vector_type(4)));
typedef __bf16 bf16x2 __attribute__((ext_vector_type(2)));
typedef float f32x16 __attribute__((ext_vector_type(16)));
typedef float f32x4 __attribute__((ext_vector_type(4)));
typedef float f32x2 __attribute__((ext_vector_type(2)));
typedef unsigned u32x4 __attribute__((ext_vector_type(4)));
typedef unsigned u32x2 __attribute__((ext_vector_type(2)));
#define LAS __attribute__((address_space(3)))
#define GAS __attribute__((address_space(1)))
#define DI __device__ __forceinline__
#define SB() __builtin_amdgcn_sched_barrier(0)
#define LGKM(n) do { __builtin_amdgcn_s_waitcnt(0xC07F | ((n) << 8)); SB(); } while (0)
#define IC(n) std::integral_constant<int, n>{}
typedef __amdgpu_buffer_rsrc_t rsrc_t;

constexpr int SQ = 112, DM = 1024, NH = 32, SK = 1024, DH = 128;
constexpr size_t WS_O = 0;
constexpr size_t WS_X = (size_t)16 << 20;
constexpr size_t WS_F = (size_t)48 << 20;
constexpr size_t WS_G = WS_F + 131072;
constexpr size_t WS_E = WS_G + 524288;
constexpr size_t WS_ST = (size_t)64 << 20;
constexpr float QSCALE = 0.08838834764831845f * 1.4426950408889634f;
constexpr float THR = 8.0f;
constexpr unsigned SPIN = 1u << 18;

#define NOUT1 24
#define NOUT2 24
#define MGR 2
#define SLP1 0
#define PB1 97
#define PA1 1650
#define PB2 113
#define PA2 3210
#define HSLP 0
#define PHI (((rr * 5) & 31) * 3 - 96)
#define GAP1 0
#define GAP2 0
#define GEND 30
#define RDN 22
#define QW 8
#define NRT 3
#define RTILES {2, 7, 12, 0, 0, 0}
#define RBURST {3, 7, 10, 99, 99, 99}
#define CSTAMP(kern, idx) do {} while (0)
#define STAMP(kern, idx) do {} while (0)

DI rsrc_t mkrsrc(const void* p, unsigned bytes) { return __builtin_amdgcn_make_buffer_rsrc((void*)p, (short)0, (int)bytes, 0x00020000); }
template <int POL> DI void dma16(unsigned mb, unsigned mi, unsigned voff, rsrc_t srd, unsigned sb, unsigned si) {
  unsigned t;
  if constexpr (POL == 1)      asm volatile("s_add_u32 m0, %1, %2\n\ts_add_u32 %0, %5, %6\n\tbuffer_load_dwordx4 %3, %4, %0 offen nt lds"  : "=&s"(t) : "s"(mb), "n"(mi), "v"(voff), "s"(srd), "s"(sb), "n"(si) : "memory", "scc");
  else if constexpr (POL == 2) asm volatile("s_add_u32 m0, %1, %2\n\ts_add_u32 %0, %5, %6\n\tbuffer_load_dwordx4 %3, %4, %0 offen sc1 lds" : "=&s"(t) : "s"(mb), "n"(mi), "v"(voff), "s"(srd), "s"(sb), "n"(si) : "memory", "scc");
  else                         asm volatile("s_add_u32 m0, %1, %2\n\ts_add_u32 %0, %5, %6\n\tbuffer_load_dwordx4 %3, %4, %0 offen lds"     : "=&s"(t) : "s"(mb), "n"(mi), "v"(voff), "s"(srd), "s"(sb), "n"(si) : "memory", "scc");
}
DI void ld16nt(u32x4& d, unsigned voff, rsrc_t srd, unsigned sb, unsigned si) {
  unsigned t;
  asm volatile("s_add_u32 %1, %4, %5\n\tbuffer_load_dwordx4 %0, %2, %3, %1 offen nt" : "=v"(d), "=&s"(t) : "v"(voff), "s"(srd), "s"(sb), "n"(si) : "memory", "scc");
}
DI void landed(u32x4 (&r)[8]) { asm volatile("" : "+v"(r[0]), "+v"(r[1]), "+v"(r[2]), "+v"(r[3]), "+v"(r[4]), "+v"(r[5]), "+v"(r[6]), "+v"(r[7])); }
template <int N> DI void wait_bar() { asm volatile("s_waitcnt vmcnt(%0) lgkmcnt(0)\n\ts_barrier" ::"n"(N) : "memory"); }
template <int N> DI void wait_vm() { asm volatile("s_waitcnt vmcnt(%0)" ::"n"(N) : "memory"); }
DI void bar() { asm volatile("s_waitcnt lgkmcnt(0)\n\ts_barrier" ::: "memory"); }
DI void rawbar() { asm volatile("s_barrier" ::: "memory"); }
DI bf16x8 lds128(unsigned a) { return *(const LAS bf16x8*)(size_t)a; }
DI bf16x4 ldstr(unsigned a) { return __builtin_amdgcn_ds_read_tr16_b64_v4bf16((LAS bf16x4*)(size_t)a); }
DI bf16x8 cat(bf16x4 a, bf16x4 b) { return __builtin_shufflevector(a, b, 0, 1, 2, 3, 4, 5, 6, 7); }
DI f32x16 mfma32(bf16x8 a, bf16x8 b, f32x16 c) { return __builtin_amdgcn_mfma_f32_32x32x16_bf16(a, b, c, 0, 0, 0); }
DI f32x4 mfma16(bf16x8 a, bf16x8 b, f32x4 c) { return __builtin_amdgcn_mfma_f32_16x16x32_bf16(a, b, c, 0, 0, 0); }
DI void mac16(f32x4& c, bf16x8 a, bf16x8 b) { asm("v_mfma_f32_16x16x32_bf16 %0, %1, %2, %0" : "+v"(c) : "v"(a), "v"(b)); }
DI unsigned cvtpk(float lo, float hi) { f32x2 v = {lo, hi}; return __builtin_bit_cast(unsigned, __builtin_convertvector(v, bf16x2)); }
DI u32x4 pack8(const f32x16& a, int o, float s) {
  u32x4 w = {cvtpk(a[o] * s, a[o + 1] * s), cvtpk(a[o + 2] * s, a[o + 3] * s), cvtpk(a[o + 4] * s, a[o + 5] * s), cvtpk(a[o + 6] * s, a[o + 7] * s)};
  return w;
}
DI float fadd(float a, float b) { float r; asm("v_add_f32_e32 %0, %1, %2" : "=v"(r) : "v"(a), "v"(b)); return r; }
DI void vadd(unsigned& a, int d) { asm volatile("v_add_u32_e32 %0, %1, %0" : "+v"(a) : "s"(d)); }
DI float max2f(float a, float b) { return __builtin_elementwise_maximum(a, b); }
DI float max3f(float a, float b, float c) { return max2f(max2f(a, b), c); }
DI float xrow_max(float x) {
  auto a = __builtin_amdgcn_permlane16_swap(__float_as_uint(x), __float_as_uint(x), false, false);
  x = max2f(__uint_as_float(a[0]), __uint_as_float(a[1]));
  auto b = __builtin_amdgcn_permlane32_swap(__float_as_uint(x), __float_as_uint(x), false, false);
  return max2f(__uint_as_float(b[0]), __uint_as_float(b[1]));
}
DI float xhalf_max(float x) { auto r = __builtin_amdgcn_permlane32_swap(__float_as_uint(x), __float_as_uint(x), false, false); return max2f(__uint_as_float(r[0]), __uint_as_float(r[1])); }
DI float xhalf_sum(float x) { auto r = __builtin_amdgcn_permlane32_swap(__float_as_uint(x), __float_as_uint(x), false, false); return __uint_as_float(r[0]) + __uint_as_float(r[1]); }
DI void st_flag(char* p, u64 v) { __hip_atomic_store((GAS u64*)p, v, __ATOMIC_RELAXED, __HIP_MEMORY_SCOPE_AGENT); }
DI u64 ld_flag(const char* p) { return __hip_atomic_load((GAS u64*)p, __ATOMIC_RELAXED, __HIP_MEMORY_SCOPE_AGENT); }
DI void poll(const char* p, bool on, u64 tag) {
#pragma unroll 1
  for (unsigned spin = 0; spin < SPIN; ++spin) {
    const u64 f = on ? ld_flag(p) : tag;
    if (__all(f == tag)) break;
    __builtin_amdgcn_s_sleep(1);
  }
  asm volatile("" ::: "memory");
}

constexpr unsigned KVA = 0, KVB = 65536, KVC = 98304, KVD = 32768;
constexpr unsigned QI1 = 98304, QI2 = 131072;
struct Ring { unsigned b0, b1, b2, w0, w1, w2; DI void rotate() { const unsigned t = b0, u = w0; b0 = b1; b1 = b2; b2 = t; w0 = w1; w1 = w2; w2 = u; } };
constexpr Ring QRING = {118784, 0, 73728, 118784 + 28672, 28672, 73728 + 28672};
constexpr Ring ORING = {16384, 73728, 45056, 131072, 147456, 0};

template <bool ISQ> struct Gemm {
  struct Frag { bf16x8 a[4], b[7]; };
  unsigned abase, bbase;
  DI Gemm(unsigned lds0, unsigned cw) {
    const unsigned lane = threadIdx.x & 63, l4 = lane >> 4, l15 = lane & 15, q4 = (lane >> 2) & 3, pp = lane & 3;
    abase = lds0 + (ISQ ? 8 * l4 + q4 : 4 * l4 + q4) * 256 + (((4 * (cw & 1)) ^ (ISQ ? (q4 << 1) | (l4 & 1) : 4 * (l4 & 1) + q4)) << 5) + pp * 8;
    bbase = lds0 + (cw >> 1) * 14336 + (ISQ ? l15 * 128 + ((l4 ^ (l15 >> 1)) << 4) : lane * 16);
  }
  struct Half { bf16x4 a[8]; };
  DI void rd(Frag& f, Half& h, int i, unsigned bs, unsigned ws, int s) const {
    if (i < 8) { h.a[i] = ldstr(((abase + ws) ^ ((i >> 1) << 5)) + s * 8192 + (i & 1) * (ISQ ? 1024 : 4096)); if (i & 1) f.a[i >> 1] = cat(h.a[i - 1], h.a[i]); }
    else if (i < 15) { const int j = i - 8; f.b[j] = ISQ ? lds128(((bbase + bs) ^ (s << 6)) + j * 2048) : lds128(bbase + bs + s * 7168 + j * 1024); }
  }
  template <bool RD, class Fin> DI void half(f32x4 (&acc)[7][4], const Frag& c, Frag& n, unsigned bs, unsigned ws, int s, Fin fin) const {
    Half h;
#pragma unroll
    for (int m = 0; m < 28; ++m) {
      acc[m >> 2][m & 3] = mfma16(c.a[m & 3], c.b[m >> 2], acc[m >> 2][m & 3]);
      SB();
      if (m >= 6) { fin(m - 6); SB(); }
      if constexpr (RD) { if (m < RDN) { if (RDN == 15) rd(n, h, m, bs, ws, s); else if ((m % 3) != 2) rd(n, h, m - m / 3, bs, ws, s); } SB(); }
    }
  }
  template <class Fin> DI void run(f32x4 (&acc)[7][4], Ring r, Fin fin) const {
    auto none = [](int) {};
    Frag f0, f1;
    {
      Half h;
#pragma unroll
      for (int i = 0; i < 15; ++i) rd(f0, h, i, r.b0, r.w0, 0);
    }
#pragma unroll 1
    for (unsigned v = 0; v < 15; ++v) {
      LGKM(0);
      half<true>(acc, f0, f1, r.b0, r.w0, 1, none);
      LGKM(0); rawbar(); SB();
      half<true>(acc, f1, f0, r.b1, r.w1, 0, none);
      if (ISQ && HSLP) __builtin_amdgcn_s_sleep(HSLP);
      r.rotate();
    }
    LGKM(0);
    half<true>(acc, f0, f1, r.b0, r.w0, 1, none);
    LGKM(0); rawbar(); SB();
    half<false>(acc, f1, f0, 0, 0, 0, fin);
#pragma unroll
    for (int m = 22; m < 28; ++m) fin(m);
  }
};
template <bool ISQ> struct GemmLoad {
  static constexpr unsigned WROW = ISQ ? 8192 : 2048;
  rsrc_t srdB, srdW;
  unsigned voffW, vB0, lds0, lw, hh, p;
  DI GemmLoad(rsrc_t b, rsrc_t w, unsigned lds0_, unsigned lw_, unsigned hh_, unsigned p_) : srdB(b), srdW(w), lds0(lds0_), lw(lw_), hh(hh_), p(p_) {
    const unsigned lane = threadIdx.x & 63, l4 = lane >> 4, l15 = lane & 15;
    voffW = l4 * WROW + ((l15 ^ (ISQ ? (l4 << 2) | (lw & 2) : (((lw & 1) << 2) | l4) << 1)) << 4);
    vB0 = ISQ ? (lane >> 3) * 2048 + ((((lane & 7) ^ l4) << 4) ^ ((lw & 1) << 6)) : lane * 16;
  }
  DI unsigned kk(unsigned v) const {
    if (ISQ) return (v & 8) | ((v + hh + 2 * p) & 7);
    unsigned j = v + ((7 * p) >> 1);
    j = j >= 16 ? j - 14 : j;
    return (2 * hh + (v < 2 ? v : j)) & 15;
  }
  DI void w(unsigned v, unsigned slot) const {
#pragma unroll
    for (unsigned i = 0; i < 4; ++i) dma16<0>(lds0 + slot + lw * 1024, i * 4096, voffW, srdW, (kk(v) * 64 + 4 * lw) * WROW, 16 * i * WROW);
  }
  DI void b(unsigned v, unsigned slot) const {
    if constexpr (ISQ) {
#pragma unroll
      for (unsigned i = 0; i < 7; ++i) dma16<0>(lds0 + slot + lw * 1024, i * 4096, vB0, srdB, lw * 16384 + kk(v) * 128, i * 65536);
    } else {
#pragma unroll
      for (unsigned i = 0; i < 3; ++i) dma16<2>(lds0 + slot + lw * 1024, i * 4096, vB0, srdB, kk(v) * 14336 + lw * 1024, i * 4096);
      const unsigned x = lw < 2 ? 12288 + lw * 1024 : 14336 + 12288 + (3 - lw) * 1024;
      dma16<2>(lds0 + slot + x, 0, vB0, srdB, kk(v) * 14336 + x + (lw < 2 ? 0 : 32 * 28672 - 14336), 0);
#pragma unroll
      for (unsigned i = 0; i < 3; ++i) dma16<2>(lds0 + slot + (3 - lw) * 1024, 14336 + i * 4096, vB0, srdB, kk(v) * 14336 + (3 - lw) * 1024, 32 * 28672 + i * 4096);
    }
  }
  DI void tile(unsigned v, unsigned bs, unsigned ws) const { w(v, ws); b(v, bs); }
};

struct Score { f32x4 s[2][2]; };
struct Attn {
  f32x4 o[8][2], ls[2], negm[2];
  Score sa, sb;
  bf16x8 qf[4][2], kf[8], vf[8], ones;
  bf16x4 vh;
  float mref[2];
  unsigned ka[4], va[8];
  DI void rd_k(int f, int ofs) { kf[f] = lds128(ka[f >> 1] + ofs + (f & 1) * 4096); }
  DI void rd_v(int i, int ofs) { const bf16x4 x = ldstr(va[i >> 1] + ofs + (i & 1) * 4096); if (i & 1) vf[i >> 1] = cat(vh, x); else vh = x; }
  DI void qk(Score& n, int i) {
    const int f = i >> 1, b = i & 1;
    if (f < 2) asm("v_mfma_f32_16x16x32_bf16 %0, %1, %2, %3" : "=&v"(n.s[f][b]) : "v"(kf[f]), "v"(qf[0][b]), "v"(negm[b]));
    else mac16(n.s[f & 1][b], kf[f], qf[f >> 1][b]);
    if (i == 7) asm volatile("" :: "v"(negm[0]), "v"(negm[1]));
  }
  DI void settle() {
    asm volatile("s_nop 7" : "+v"(o[0][0]), "+v"(o[0][1]), "+v"(o[1][0]), "+v"(o[1][1]), "+v"(o[2][0]), "+v"(o[2][1]), "+v"(o[3][0]), "+v"(o[3][1]), "+v"(o[4][0]), "+v"(o[4][1]),
                 "+v"(o[5][0]), "+v"(o[5][1]), "+v"(o[6][0]), "+v"(o[6][1]), "+v"(o[7][0]), "+v"(o[7][1]), "+v"(ls[0]), "+v"(ls[1]));
  }
  template <bool FIRST> DI void rescale(Score& n, bool grow, bool pad) {
    if (FIRST || __builtin_expect(grow, 0)) {
      if constexpr (!FIRST) settle();
#pragma unroll
      for (int b = 0; b < 2; ++b) {
        const f32x4 &x = n.s[0][b], &y = n.s[1][b];
        const float mx = xrow_max(max2f(max3f(max3f(x[0], x[1], x[2]), x[3], y[0]), max3f(y[1], y[2], y[3])));
        const float d = FIRST ? ((b && pad) ? 1e30f : mx) : max2f(mx, 0.f);
        mref[b] += d;
        n.s[0][b] -= d; n.s[1][b] -= d;
        negm[b] = f32x4{-mref[b], -mref[b], -mref[b], -mref[b]};
        if constexpr (!FIRST) {
          const float f = __builtin_amdgcn_exp2f(-d);
          ls[b] *= f;
#pragma unroll
          for (int c = 0; c < 8; ++c) o[c][b] *= f;
        }
      }
      asm volatile("s_nop 1" : "+v"(negm[0]), "+v"(negm[1]));
    }
  }
  DI void advance(int delta) {
#pragma unroll
    for (int i = 0; i < 4; ++i) ka[i] += delta;
#pragma unroll
    for (int i = 0; i < 8; ++i) va[i] += delta;
  }
  DI void init(unsigned lds0, unsigned w4, unsigned qimg, unsigned kslot, unsigned slot0) {
    const unsigned lane = threadIdx.x & 63, l15 = lane & 15, l4 = lane >> 4, q4 = (lane >> 2) & 3, pp = lane & 3;
    const unsigned kb = l15 * 256 + ((l4 ^ l15) << 4);
    const unsigned vb = (4 * l4 + q4) * 256 + ((4 * (l4 & 1) + q4) << 5) + pp * 8;
    const bool pad = w4 == 3;
#pragma unroll
    for (int i = 0; i < 4; ++i) {
      ka[i] = lds0 + kslot + 16384 + (kb ^ (i << 6));
      const unsigned a = lds0 + qimg + w4 * 8192 + (kb ^ (i << 6));
      qf[i][0] = lds128(a); qf[i][1] = lds128(a + (pad ? 0 : 4096));
      rd_k(2 * i, 0); rd_k(2 * i + 1, 0);
      SB();
    }
#pragma unroll
    for (int c = 0; c < 8; ++c) { o[c][0] = f32x4{}; o[c][1] = f32x4{}; va[c] = lds0 + kslot + (vb ^ (c << 5)); }
    if (pad) {
#pragma unroll
      for (int i = 0; i < 4; ++i) qf[i][1] = bf16x8{};
    }
    ones = __builtin_bit_cast(bf16x8, u32x4{0x3f803f80u, 0x3f803f80u, 0x3f803f80u, 0x3f803f80u});
    ls[0] = ls[1] = negm[0] = negm[1] = f32x4{}; mref[0] = mref[1] = 0.f;
    SB();
    asm volatile("s_nop 1" : "+v"(negm[0]), "+v"(negm[1]), "+v"(ones), "+v"(qf[0][1]));
#pragma unroll
    for (int i = 0; i < 16; ++i) { qk(sa, i); SB(); }
#pragma unroll
    for (int f = 0; f < 8; ++f) rd_k(f, 8192);
    asm volatile("s_nop 7" : "+v"(sa.s[0][0]), "+v"(sa.s[0][1]), "+v"(sa.s[1][0]), "+v"(sa.s[1][1]));
    rescale<true>(sa, true, pad);
    advance(slot0 - kslot);
    SB();
  }
  template <bool NEXT, bool RDK, bool ADV> DI void sub(Score& c, Score& n, int ofs, int delta) {
    u32x4 p[2];
    LGKM(0);
#pragma unroll
    for (int i = 0; i < 16; ++i) {
      if constexpr (NEXT) { qk(n, i); SB(); }
      if (i & 1) { rd_v(i >> 1, ofs); SB(); }
      f32x4& x = c.s[(i >> 2) & 1][i >> 3];
      x[i & 3] = __builtin_amdgcn_exp2f(x[i & 3]);
      if (i >= 2 && !(i & 1)) { const int k = i - 2; const f32x4& y = c.s[(k >> 2) & 1][k >> 3]; p[k >> 3][(k >> 1) & 3] = cvtpk(y[k & 3], y[(k & 3) + 1]); }
      SB();
    }
    float m0, m1;
    bool grow = false;
#pragma unroll
    for (int i = 0; i < 18; ++i) {
      if (i == 0) { LGKM(4); p[1][3] = cvtpk(c.s[1][1][2], c.s[1][1][3]); SB(); }
      if (i == 4) LGKM(RDK ? 5 : 4);
      if (RDK && i == 8) LGKM(8);
      if (RDK && i == 12) LGKM(4);
      if (i < 16) mac16(o[i >> 1][i & 1], vf[i >> 1], __builtin_bit_cast(bf16x8, p[i & 1]));
      else mac16(ls[i & 1], ones, __builtin_bit_cast(bf16x8, p[i & 1]));
      SB();
      if (i < 8) rd_v(8 + i, ofs);
      if constexpr (RDK) { if (i >= 3 && i < 11) rd_k(i - 3, ofs); }
      if constexpr (NEXT) {
        if (i == 1) m0 = max3f(n.s[0][0][0], n.s[0][0][1], n.s[0][0][2]);
        if (i == 2) m1 = max3f(n.s[0][0][3], n.s[0][1][0], n.s[0][1][1]);
        if (i == 8) m0 = max3f(m0, n.s[0][1][2], n.s[0][1][3]);
        if (i == 9) m1 = max3f(m1, n.s[1][0][0], n.s[1][0][1]);
        if (i == 10) m0 = max3f(m0, n.s[1][0][2], n.s[1][0][3]);
        if (i == 11) { m1 = max3f(m1, n.s[1][1][0], n.s[1][1][1]); m0 = max3f(m0, n.s[1][1][2], n.s[1][1][3]); }
        if (i == 12) m0 = max2f(m0, m1);
        if (i == 13) grow = __any(m0 > THR);
      }
      if constexpr (ADV) {
        if (i >= 12 && i < 16) { vadd(va[2 * i - 24], delta); vadd(va[2 * i - 23], delta); }
        if (i >= 16) { vadd(ka[2 * i - 32], delta); vadd(ka[2 * i - 31], delta); }
      }
      SB();
    }
    if constexpr (NEXT) rescale<false>(n, grow, false);
  }
  template <int SLP> DI void run() {
    unsigned s0 = KVA, s1 = KVB, s2 = KVC, s3 = KVD;
    wait_bar<8>();
#pragma unroll 1
    for (unsigned n = 0; n < 15; ++n) {
      sub<true, true, false>(sa, sb, 0, 0);
      sub<true, true, true>(sb, sa, 8192, s1 - s0);
      if (SLP) __builtin_amdgcn_s_sleep(SLP);
      const unsigned t = s0; s0 = s1; s1 = s2; s2 = s3; s3 = t;
      wait_bar<0>();
    }
    sub<true, false, false>(sa, sb, 0, 0);
    sub<false, false, false>(sb, sa, 8192, 0);
    settle();
  }
  DI void finish(u32x4 (&of)[8]) {
#pragma unroll
    for (int b = 0; b < 2; ++b) {
      const float inv = 1.f / ls[b][0];
#pragma unroll
      for (int s = 0; s < 4; ++s) {
        const f32x4 x = o[2 * s][b] * inv, y = o[2 * s + 1][b] * inv;
        of[2 * s + b] = u32x4{cvtpk(x[0], x[1]), cvtpk(x[2], x[3]), cvtpk(y[0], y[1]), cvtpk(y[2], y[3])};
      }
    }
  }
};
DI void publish_o(const u32x4 (&of)[8], rsrc_t srdO, unsigned so, unsigned w4) {
  const unsigned vo = so + 2 * w4 * 1024 + (threadIdx.x & 63) * 16, vo1 = w4 == 3 ? 0x80000000u : vo + 1024;
#pragma unroll
  for (int s = 0; s < 4; ++s) {
    __builtin_amdgcn_raw_buffer_store_b128(of[2 * s], srdO, vo + s * 7168, 0, 16);
    __builtin_amdgcn_raw_buffer_store_b128(of[2 * s + 1], srdO, vo1 + s * 7168, 0, 16);
  }
}
template <int T> DI void put_o(const u32x4 (&of)[8], unsigned base, unsigned w4) {
  const unsigned a = base + 2 * w4 * 1024 + (threadIdx.x & 63) * 16;
  *(LAS u32x4*)(size_t)a = of[4 * T]; *(LAS u32x4*)(size_t)(a + 7168) = of[4 * T + 2];
  if (w4 < 3) { *(LAS u32x4*)(size_t)(a + 1024) = of[4 * T + 1]; *(LAS u32x4*)(size_t)(a + 8192) = of[4 * T + 3]; }
}

constexpr int NR = NRT;
constexpr int RT[6] = RTILES;
constexpr int RB[6] = RBURST;
constexpr unsigned mask_of(const int (&a)[6]) { unsigned m = 0; for (int i = 0; i < NR; ++i) m |= 1u << a[i]; return m; }
constexpr unsigned RMASK = mask_of(RT), BMASK = mask_of(RB);

__global__ __launch_bounds__(512, 2)
void k_fused(const u16* __restrict__ xA, const u16* __restrict__ kA, const u16* __restrict__ vA, const u16* __restrict__ wqA, const u16* __restrict__ woA,
             const u16* __restrict__ xB, const u16* __restrict__ kB, const u16* __restrict__ vB, const u16* __restrict__ wqB, const u16* __restrict__ woB,
             char* __restrict__ ws, u16* __restrict__ out) {
  extern __shared__ __attribute__((aligned(16))) char smem[];
  const unsigned lds0 = (unsigned)(size_t)(LAS char*)smem;
  const unsigned lane = threadIdx.x & 63, wid = __builtin_amdgcn_readfirstlane(threadIdx.x >> 6), w4 = wid & 3;
  const unsigned bid = blockIdx.x, xcd = bid & 7, rr = bid >> 3, br = xcd & 1, g = xcd >> 1, hh = rr & 7, p = rr >> 3;
  const unsigned head = 8 * g + hh;
#define KROT 0
  const unsigned krot = KROT;
  STAMP(0, 0); CSTAMP(0, 4);
  const unsigned t0 = (unsigned)wall_clock64() + PHI;
  int tgate = 0;
  auto not_before = [&](int ticks, int gap) {
    ticks = ticks > tgate + gap ? ticks : tgate + gap;
    while ((tgate = (int)((unsigned)wall_clock64() - t0)) < ticks) __builtin_amdgcn_s_sleep(1);
  };
  const char* const kptr = (const char*)(br ? kB : kA) + (size_t)(2 * p * NH + head) * (SK * DH * 2);
  const char* const vptr = (const char*)(br ? vB : vA) + (size_t)(2 * p * NH + head) * (SK * DH * 2);
  const rsrc_t srdX = mkrsrc((const char*)(br ? xB : xA) + (size_t)p * (2 * SQ * DM * 2), 2 * SQ * DM * 2);
  const rsrc_t srdWq = mkrsrc((const char*)(br ? wqB : wqA) + head * 256, DM * NH * DH * 2 - head * 256);
  const rsrc_t srdWo = mkrsrc((const char*)(br ? woB : woA) + (size_t)g * (1024 * 2048) + hh * 256, 1024 * 2048 - hh * 256);
  const rsrc_t srdO = mkrsrc(ws + WS_O + (size_t)((br * 8 + 2 * p) * 32 + 8 * g) * 28672, (32 + 8) * 28672);
  const rsrc_t srdE = mkrsrc(ws + WS_X, 256 * 65536);
  char* const epoch = ws + WS_E + (br * 4 + p) * 64;
  char* const fam = ws + WS_F + ((br * 4 + g) * 8 + 2 * p) * 2048;
  u16* const outp = out + (size_t)((2 * p + (w4 >> 1)) * SQ) * 2048 + br * 1024 + hh * 128 + (w4 & 1) * 64;
  asm volatile("" :: "s"(kptr), "s"(vptr), "s"(srdX), "s"(srdWq), "s"(srdWo), "s"(srdO), "s"(srdE), "s"(outp));
  u64 tag;
  auto load_tag = [&]() { asm volatile("s_load_dwordx2 %0, %1, 0x0\n\ts_waitcnt lgkmcnt(0)" : "=&s"(tag) : "s"(epoch) : "memory"); tag += 1; };
  const unsigned l4 = lane >> 4, l15 = lane & 15;
  const unsigned voffV = l4 * 256 + ((l15 ^ ((((w4 & 1) << 2) | l4) << 1)) << 4), voffK = l4 * 256 + ((l15 ^ ((w4 << 2) | l4)) << 4);
  constexpr unsigned KVBYTES = NH * SK * DH * 2 + SK * DH * 2;
  const rsrc_t srdV = mkrsrc(vptr, KVBYTES);
  auto so_k = [&](int n) { return ((n + 1) >> 4) * (NH * SK * DH * 2) + ((n + 1 + krot) & 15) * 16384 + w4 * 1024; };
  auto so_v = [&](int n) { return (n >> 4) * (NH * SK * DH * 2) + ((n + krot) & 15) * 16384 + w4 * 1024; };
  auto issue_k = [&](int n, unsigned slot) {
    const rsrc_t r = mkrsrc(kptr, n < 31 ? KVBYTES : 0);
#pragma unroll
    for (unsigned i = 0; i < 4; ++i) dma16<1>(lds0 + slot + w4 * 1024, 16384 + i * 4096, voffK, r, so_k(n), i * 4096);
  };
  auto issue_kv = [&](int n, unsigned slot) {
    issue_k(n, slot);
#pragma unroll
    for (unsigned i = 0; i < 4; ++i) dma16<1>(lds0 + slot + w4 * 1024, i * 4096, voffV, srdV, so_v(n), i * 4096);
  };
  auto issue_kv_m = [&](auto NO, int n, unsigned slot) {
    constexpr int N = decltype(NO)::value;
    const rsrc_t r = mkrsrc(kptr, n < 31 ? KVBYTES : 0);
#pragma unroll
    for (unsigned i = 0; i < 4; ++i) { if (i % MGR == 0) wait_vm<N - MGR>(); dma16<1>(lds0 + slot + w4 * 1024, 16384 + i * 4096, voffK, r, so_k(n), i * 4096); }
#pragma unroll
    for (unsigned i = 0; i < 4; ++i) { if (i % MGR == 0) wait_vm<N - MGR>(); dma16<1>(lds0 + slot + w4 * 1024, i * 4096, voffV, srdV, so_v(n), i * 4096); }
  };
  const unsigned bid0 = bid & ~6u;

  if (wid < 4) {
    Attn at;
    load_tag();
    {
      f32x4 acc[7][4] = {};
      const Gemm<true> gm(lds0, w4);
      rawbar(); rawbar();
      const unsigned qa = lds0 + ((w4 >> 1) ? QI2 : QI1) + l15 * 256 + (l4 & 1) * 8 + (((8 * (w4 & 1) + (l4 >> 1)) ^ l15) << 4);
      gm.run(acc, QRING, [&](int m) {
        const f32x4 x = acc[m >> 2][m & 3] * QSCALE;
        *(LAS u32x2*)(size_t)((qa ^ ((m & 3) << 5)) + (m >> 2) * 4096) = u32x2{cvtpk(x[0], x[1]), cvtpk(x[2], x[3])};
      });
    }
    bar();
    STAMP(0, 1); CSTAMP(0, 5);
    at.init(lds0, w4, QI1, KVD, KVA);
    at.template run<SLP1>();
    STAMP(0, 2); CSTAMP(0, 6);
    const GemmLoad<false> gl(srdO, srdWo, lds0, w4, hh, p);
    bar();
    issue_kv(18, KVC);
    u32x4 of[8];
    at.finish(of);
    {
      unsigned ks = KVD, ks1 = KVA, ks2 = KVB, ks3 = KVC;
#pragma unroll 1
      for (unsigned n = 17; n < 30; ++n) {
        wait_vm<8>();
        not_before(PA2 + (n - 16) * PB2, n < GEND ? GAP2 : 0);
        bar();
        issue_kv_m(IC(NOUT2), n + 2, ks);
        const unsigned t = ks; ks = ks1; ks1 = ks2; ks2 = ks3; ks3 = t;
      }
    }
    gl.w(0, ORING.w0);
    put_o<0>(of, lds0 + ORING.b0, w4);
    wait_bar<12>();
    put_o<1>(of, lds0 + ORING.b1, w4);
    publish_o(of, srdO, hh * 28672, w4);
    wait_bar<12>();
    gl.w(1, ORING.w1); gl.w(2, ORING.w2);
    wait_vm<8>();
    if (lane == 0) st_flag(fam + (hh * 4 + w4) * 64, tag);
    f32x4 acc[7][4] = {};
    const Gemm<false> gm(lds0, w4);
    rawbar();
    STAMP(1, 0);
    wait_bar<0>();
    STAMP(1, 2);
    const unsigned xo = bid0 * 65536 + g * 16384 + w4 * 4096 + lane * 16;
    gm.run(acc, ORING, [&](int m) {
      const int j = m >> 2, n = m & 3;
      if ((n & 1) && (unsigned)(j >> 1) != g) {
        const f32x4 a = acc[j][n - 1], b = acc[j][n];
        const u32x4 w = {cvtpk(a[0], a[1]), cvtpk(a[2], a[3]), cvtpk(b[0], b[1]), cvtpk(b[2], b[3])};
        __builtin_amdgcn_raw_buffer_store_b128(w, srdE, xo + (j >> 1) * 131072 + ((j & 1) * 2 + (n >> 1)) * 1024, 0, 16);
      }
    });
    STAMP(1, 3);
    f32x4 t[2][4];
#pragma unroll
    for (int n = 0; n < 4; ++n) {
      t[0][n] = g == 0 ? acc[0][n] : g == 1 ? acc[2][n] : g == 2 ? acc[4][n] : acc[6][n];
      t[1][n] = g == 0 ? acc[1][n] : g == 1 ? acc[3][n] : acc[5][n];
    }
    wait_vm<0>();
    STAMP(1, 4);
    if (lane < 4) st_flag(ws + WS_G + (((bid0 + 2 * lane) * 4 + w4) * 4 + g) * 64, tag);
    poll(ws + WS_G + ((bid * 4 + w4) * 4 + (lane & 3)) * 64, lane < 4, tag);
    u32x4 e[3][2][2];
#pragma unroll
    for (int jj = 0; jj < 2; ++jj)
#pragma unroll
      for (int n = 0; n < 2; ++n)
#pragma unroll
        for (unsigned sr = 0; sr < 3; ++sr)
          e[sr][jj][n] = __builtin_amdgcn_raw_buffer_load_b128(srdE, (jj && g == 3) ? 0x80000000u : lane * 16 + (jj * 2 + n) * 1024, bid * 65536 + ((g + sr + 1) & 3) * 16384 + w4 * 4096, 16);
#pragma unroll
    for (int jj = 0; jj < 2; ++jj) {
      u16* o = outp + (size_t)((2 * g + jj) * 16 + l15) * 2048 + l4 * 4;
#pragma unroll
      for (int n = 0; n < 4; ++n) {
        f32x4 v = t[jj][n];
#pragma unroll
        for (int sr = 0; sr < 3; ++sr) {
          const unsigned x = e[sr][jj][n >> 1][2 * (n & 1)], y = e[sr][jj][n >> 1][2 * (n & 1) + 1];
          v += f32x4{__uint_as_float(x << 16), __uint_as_float(x & 0xffff0000u), __uint_as_float(y << 16), __uint_as_float(y & 0xffff0000u)};
        }
        u32x2 w = {cvtpk(v[0], v[1]), cvtpk(v[2], v[3])};
        if (!(jj && g == 3)) *(GAS u32x2*)(o + n * 16) = w;
      }
    }
    if (g == 0 && hh == 0 && wid == 0 && lane == 0) st_flag(epoch, tag);
    STAMP(1, 5); STAMP(1, 6); STAMP(1, 7); STAMP(0, 3);
  } else {
    Attn at;
    {
      u32x4 rk[6][8];
      const rsrc_t srdK = mkrsrc(kptr, KVBYTES);
      auto fetch = [&](auto RI) {
        constexpr int r = decltype(RI)::value;
        if constexpr (r < NR) {
#pragma unroll
          for (int i = 0; i < 4; ++i) ld16nt(rk[r][4 + i], voffK, srdK, so_k(RT[r]), i * 4096);
#pragma unroll
          for (int i = 0; i < 4; ++i) ld16nt(rk[r][i], voffV, srdV, so_v(RT[r]), i * 4096);
        }
      };
      auto put = [&](auto RI, unsigned slot) {
        constexpr int r = decltype(RI)::value;
        if constexpr (r < NR) {
#pragma unroll
          for (int i = 0; i < 8; ++i) *(LAS u32x4*)(size_t)(lds0 + slot + w4 * 1024 + lane * 16 + i * 4096) = rk[r][i];
        }
      };
      const GemmLoad<true> gl(srdX, srdWq, lds0, w4, hh, p);
      auto qload = [&](auto PHASE) {
        constexpr int PH = decltype(PHASE)::value;
        Ring r = QRING;
        gl.tile(0, r.b0, r.w0); rawbar();
        gl.tile(1, r.b1, r.w1);
        wait_bar<11>();
        gl.tile(2, r.b2, r.w2);
#pragma unroll
        for (int v = 0; v < 13; ++v) {
          constexpr unsigned BM = BMASK << (2 + PH);
          const int nb = ((BM >> v) & 1) + ((BM >> (v + 1)) & 1);
          if (nb == 0) wait_bar<11>(); else if (nb == 1) wait_bar<19>(); else wait_bar<27>();
          gl.tile(v + 3, r.b0, r.w0);
          if (v == RB[0] + PH) fetch(IC(0)); if (v == RB[1] + PH) fetch(IC(1)); if (v == RB[2] + PH) fetch(IC(2));
          if (v == RB[3] + PH) fetch(IC(3)); if (v == RB[4] + PH) fetch(IC(4)); if (v == RB[5] + PH) fetch(IC(5));
          r.rotate();
        }
        issue_k(-1, KVD);
        if constexpr (((BMASK << PH) >> 12) & 1) wait_bar<23>(); else wait_bar<15>();
        issue_kv(0, KVA);
        wait_bar<12>();
#pragma unroll
        for (int r = 0; r < NR; ++r) landed(rk[r]);
      };
#define STAG 1
      if (hh & 1) qload(IC(STAG)); else qload(IC(0));
      rawbar();
      issue_kv(1, KVB);
      wait_bar<16>();
      STAMP(0, 1); CSTAMP(0, 5);
      wait_bar<8>();
      put(IC(0), KVC);
      unsigned ks = KVD, ks1 = KVA, ks2 = KVB, ks3 = KVC;
#pragma unroll
      for (int n = 0; n < 15; ++n) {
        const int u = n + 3;
        if ((RMASK >> u) & 1) {
          if (u == RT[1]) put(IC(1), ks); else if (u == RT[2]) put(IC(2), ks); else if (u == RT[3]) put(IC(3), ks);
          else if (u == RT[4]) put(IC(4), ks); else put(IC(5), ks);
        } else issue_kv_m(IC(NOUT1), u, ks);
        const unsigned t = ks; ks = ks1; ks1 = ks2; ks2 = ks3; ks3 = t;
        const int nd = 2 - ((RMASK >> (n + 2)) & 1) - ((RMASK >> u) & 1);
        if (nd == 2) wait_vm<16>(); else if (nd == 1) wait_vm<8>(); else wait_vm<0>();
        not_before(PA1 + (n + 1) * PB1, GAP1);
        bar();
      }
    }
    load_tag();
    at.init(lds0, w4, QI2, KVD, KVA);
    STAMP(0, 2); CSTAMP(0, 6);
    at.template run<0>();
    STAMP(1, 0);
    bar();
    u32x4 of[8];
    at.finish(of);
    publish_o(of, srdO, (32 + hh) * 28672, w4);
    put_o<0>(of, lds0 + ORING.b0 + 14336, w4); put_o<1>(of, lds0 + ORING.b1 + 14336, w4);
    const GemmLoad<false> gl(srdO, srdWo, lds0, w4, hh, p);
    bar();
    wait_vm<0>();
    if (lane == 0) st_flag(fam + 2048 + (hh * 4 + w4) * 64, tag);
    rawbar();
    Ring r = ORING;
    gl.w(3, r.w0);
    STAMP(1, 1);
    poll(fam + lane * 64, true, tag);
    STAMP(1, 2);
    gl.b(2, r.b2); gl.b(3, r.b0);
    wait_bar<7>();
    r.rotate();
    gl.tile(4, r.b0, r.w0);
    r.rotate();
#pragma unroll 1
    for (unsigned v = 2; v < 13; ++v) { wait_bar<11>(); gl.tile(v + 3, r.b0, r.w0); r.rotate(); }
    wait_bar<11>();
    wait_bar<0>();
    rawbar();
    STAMP(1, 3); STAMP(1, 4);
    STAMP(1, 5); STAMP(1, 6); STAMP(1, 7); STAMP(0, 3);
  }
}

extern "C" void kernel_launch(void* const* d_in, const int* in_sizes, int n_in, void* d_out, int out_size, void* d_ws, size_t ws_size, hipStream_t stream) {
  (void)hipFuncSetAttribute((const void*)k_fused, hipFuncAttributeMaxDynamicSharedMemorySize, 163840);
  k_fused<<<256, 512, 163840, stream>>>((const u16*)d_in[0], (const u16*)d_in[1], (const u16*)d_in[2], (const u16*)d_in[3], (const u16*)d_in[4],
                                        (const u16*)d_in[5], (const u16*)d_in[6], (const u16*)d_in[7], (const u16*)d_in[8], (const u16*)d_in[9],
                                        (char*)d_ws, (u16*)d_out);
}
```

```cpp
#include <hip/hip_runtime.h>
#include <stdint.h>
#include <type_traits>

typedef unsigned short u16;
typedef unsigned long long u64;
typedef __bf16 bf16x8 __attribute__((ext_vector_type(8)));
typedef __bf16 bf16x4 __attribute__((ext_vector_type(4)));
typedef __bf16 bf16x2 __attribute__((ext_vector_type(2)));
typedef float f32x16 __attribute__((ext_vector_type(16)));
typedef float f32x4 __attribute__((ext_vector_type(4)));
typedef float f32x2 __attribute__((ext_vector_type(2)));
typedef unsigned u32x4 __attribute__((ext_vector_type(4)));
typedef unsigned u32x2 __attribute__((ext_vector_type(2)));
#define LAS __attribute__((address_space(3)))
#define GAS __attribute__((address_space(1)))
#define DI __device__ __forceinline__
#define SB() __builtin_amdgcn_sched_barrier(0)
#define LGKM(n) do { __builtin_amdgcn_s_waitcnt(0xC07F | ((n) << 8)); SB(); } while (0)
#define IC(n) std::integral_constant<int, n>{}
typedef __amdgpu_buffer_rsrc_t rsrc_t;

constexpr int SQ = 112, DM = 1024, NH = 32, SK = 1024, DH = 128;
constexpr size_t WS_O = 0;
constexpr size_t WS_X = (size_t)16 << 20;
constexpr size_t WS_F = (size_t)48 << 20;
constexpr size_t WS_G = WS_F + 131072;
constexpr size_t WS_E = WS_G + 524288;
constexpr size_t WS_ST = (size_t)64 << 20;
constexpr float QSCALE = 0.08838834764831845f * 1.4426950408889634f;
constexpr float THR = 8.0f;
constexpr unsigned SPIN = 1u << 18;

#define NOUT1 24
#define NOUT2 24
#define MGR 2
#define SLP1 0
#define PB1 97
#define PA1 1650
#define PB2 113
#define PA2 3210
#define HSLP 0
#define PHI (((rr * 5) & 31) * 3 - 96)
#define GAP1 0
#define GAP2 0
#define GEND 30
#define RDN 22
#define QW 8
#define NRT 3
#define RTILES {2, 7, 12, 0, 0, 0}
#define RBURST {3, 7, 10, 99, 99, 99}
#define CSTAMP(kern, idx) do {} while (0)
#define STAMP(kern, idx) do {} while (0)

DI rsrc_t mkrsrc(const void* p, unsigned bytes) { return __builtin_amdgcn_make_buffer_rsrc((void*)p, (short)0, (int)bytes, 0x00020000); }
template <int POL> DI void dma16(unsigned mb, unsigned mi, unsigned voff, rsrc_t srd, unsigned sb, unsigned si) {
  unsigned t;
  if constexpr (POL == 1)      asm volatile("s_add_u32 m0, %1, %2\n\ts_add_u32 %0, %5, %6\n\tbuffer_load_dwordx4 %3, %4, %0 offen nt lds"  : "=&s"(t) : "s"(mb), "n"(mi), "v"(voff), "s"(srd), "s"(sb), "n"(si) : "memory", "scc");
  else if constexpr (POL == 2) asm volatile("s_add_u32 m0, %1, %2\n\ts_add_u32 %0, %5, %6\n\tbuffer_load_dwordx4 %3, %4, %0 offen sc1 lds" : "=&s"(t) : "s"(mb), "n"(mi), "v"(voff), "s"(srd), "s"(sb), "n"(si) : "memory", "scc");
  else                         asm volatile("s_add_u32 m0, %1, %2\n\ts_add_u32 %0, %5, %6\n\tbuffer_load_dwordx4 %3, %4, %0 offen lds"     : "=&s"(t) : "s"(mb), "n"(mi), "v"(voff), "s"(srd), "s"(sb), "n"(si) : "memory", "scc");
}
DI void ld16nt(u32x4& d, unsigned voff, rsrc_t srd, unsigned sb, unsigned si) {
  unsigned t;
  asm volatile("s_add_u32 %1, %4, %5\n\tbuffer_load_dwordx4 %0, %2, %3, %1 offen nt" : "=v"(d), "=&s"(t) : "v"(voff), "s"(srd), "s"(sb), "n"(si) : "memory", "scc");
}
DI void landed(u32x4 (&r)[8]) { asm volatile("" : "+v"(r[0]), "+v"(r[1]), "+v"(r[2]), "+v"(r[3]), "+v"(r[4]), "+v"(r[5]), "+v"(r[6]), "+v"(r[7])); }
template <int N> DI void wait_bar() { asm volatile("s_waitcnt vmcnt(%0) lgkmcnt(0)\n\ts_barrier" ::"n"(N) : "memory"); }
template <int N> DI void wait_vm() { asm volatile("s_waitcnt vmcnt(%0)" ::"n"(N) : "memory"); }
DI void bar() { asm volatile("s_waitcnt lgkmcnt(0)\n\ts_barrier" ::: "memory"); }
DI void rawbar() { asm volatile("s_barrier" ::: "memory"); }
DI bf16x8 lds128(unsigned a) { return *(const LAS bf16x8*)(size_t)a; }
DI bf16x4 ldstr(unsigned a) { return __builtin_amdgcn_ds_read_tr16_b64_v4bf16((LAS bf16x4*)(size_t)a); }
DI bf16x8 cat(bf16x4 a, bf16x4 b) { return __builtin_shufflevector(a, b, 0, 1, 2, 3, 4, 5, 6, 7); }
DI f32x16 mfma32(bf16x8 a, bf16x8 b, f32x16 c) { return __builtin_amdgcn_mfma_f32_32x32x16_bf16(a, b, c, 0, 0, 0); }
DI f32x4 mfma16(bf16x8 a, bf16x8 b, f32x4 c) { return __builtin_amdgcn_mfma_f32_16x16x32_bf16(a, b, c, 0, 0, 0); }
DI void mac16(f32x4& c, bf16x8 a, bf16x8 b) { asm("v_mfma_f32_16x16x32_bf16 %0, %1, %2, %0" : "+v"(c) : "v"(a), "v"(b)); }
DI unsigned cvtpk(float lo, float hi) { f32x2 v = {lo, hi}; return __builtin_bit_cast(unsigned, __builtin_convertvector(v, bf16x2)); }
DI u32x4 pack8(const f32x16& a, int o, float s) {
  u32x4 w = {cvtpk(a[o] * s, a[o + 1] * s), cvtpk(a[o + 2] * s, a[o + 3] * s), cvtpk(a[o + 4] * s, a[o + 5] * s), cvtpk(a[o + 6] * s, a[o + 7] * s)};
  return w;
}
DI float fadd(float a, float b) { float r; asm("v_add_f32_e32 %0, %1, %2" : "=v"(r) : "v"(a), "v"(b)); return r; }
DI void vadd(unsigned& a, int d) { asm volatile("v_add_u32_e32 %0, %1, %0" : "+v"(a) : "s"(d)); }
DI float max2f(float a, float b) { return __builtin_elementwise_maximum(a, b); }
DI float max3f(float a, float b, float c) { return max2f(max2f(a, b), c); }
DI float xrow_max(float x) {
  auto a = __builtin_amdgcn_permlane16_swap(__float_as_uint(x), __float_as_uint(x), false, false);
  x = max2f(__uint_as_float(a[0]), __uint_as_float(a[1]));
  auto b = __builtin_amdgcn_permlane32_swap(__float_as_uint(x), __float_as_uint(x), false, false);
  return max2f(__uint_as_float(b[0]), __uint_as_float(b[1]));
}
DI float xhalf_max(float x) { auto r = __builtin_amdgcn_permlane32_swap(__float_as_uint(x), __float_as_uint(x), false, false); return max2f(__uint_as_float(r[0]), __uint_as_float(r[1])); }
DI float xhalf_sum(float x) { auto r = __builtin_amdgcn_permlane32_swap(__float_as_uint(x), __float_as_uint(x), false, false); return __uint_as_float(r[0]) + __uint_as_float(r[1]); }
DI void st_flag(char* p, u64 v) { __hip_atomic_store((GAS u64*)p, v, __ATOMIC_RELAXED, __HIP_MEMORY_SCOPE_AGENT); }
DI u64 ld_flag(const char* p) { return __hip_atomic_load((GAS u64*)p, __ATOMIC_RELAXED, __HIP_MEMORY_SCOPE_AGENT); }
DI void poll(const char* p, bool on, u64 tag) {
#pragma unroll 1
  for (unsigned spin = 0; spin < SPIN; ++spin) {
    const u64 f = on ? ld_flag(p) : tag;
    if (__all(f == tag)) break;
    __builtin_amdgcn_s_sleep(1);
  }
  asm volatile("" ::: "memory");
}

constexpr unsigned KVA = 0, KVB = 65536, KVC = 98304, KVD = 32768;
constexpr unsigned QI1 = 98304, QI2 = 131072;
struct Ring { unsigned b0, b1, b2, w0, w1, w2; DI void rotate() { const unsigned t = b0, u = w0; b0 = b1; b1 = b2; b2 = t; w0 = w1; w1 = w2; w2 = u; } };
constexpr Ring QRING = {118784, 0, 73728, 118784 + 28672, 28672, 73728 + 28672};
constexpr Ring ORING = {16384, 73728, 45056, 131072, 147456, 0};

template <bool ISQ> struct Gemm {
  struct Frag { bf16x8 a[4], b[7]; };
  unsigned abase, bbase;
  DI Gemm(unsigned lds0, unsigned cw) {
    const unsigned lane = threadIdx.x & 63, l4 = lane >> 4, l15 = lane & 15, q4 = (lane >> 2) & 3, pp = lane & 3;
    abase = lds0 + (ISQ ? 8 * l4 + q4 : 4 * l4 + q4) * 256 + (((4 * (cw & 1)) ^ (ISQ ? (q4 << 1) | (l4 & 1) : 4 * (l4 & 1) + q4)) << 5) + pp * 8;
    bbase = lds0 + (cw >> 1) * 14336 + (ISQ ? l15 * 128 + ((l4 ^ (l15 >> 1)) << 4) : lane * 16);
  }
  struct Half { bf16x4 a[8]; };
  DI void rd(Frag& f, Half& h, int i, unsigned bs, unsigned ws, int s) const {
    if (i < 8) { h.a[i] = ldstr(((abase + ws) ^ ((i >> 1) << 5)) + s * 8192 + (i & 1) * (ISQ ? 1024 : 4096)); if (i & 1) f.a[i >> 1] = cat(h.a[i - 1], h.a[i]); }
    else if (i < 15) { const int j = i - 8; f.b[j] = ISQ ? lds128(((bbase + bs) ^ (s << 6)) + j * 2048) : lds128(bbase + bs + s * 7168 + j * 1024); }
  }
  template <bool RD, class Fin> DI void half(f32x4 (&acc)[7][4], const Frag& c, Frag& n, unsigned bs, unsigned ws, int s, Fin fin) const {
    Half h;
#pragma unroll
    for (int m = 0; m < 28; ++m) {
      acc[m >> 2][m & 3] = mfma16(c.a[m & 3], c.b[m >> 2], acc[m >> 2][m & 3]);
      SB();
      if (m >= 6) { fin(m - 6); SB(); }
      if constexpr (RD) { if (m < RDN) { if (RDN == 15) rd(n, h, m, bs, ws, s); else if ((m % 3) != 2) rd(n, h, m - m / 3, bs, ws, s); } SB(); }
    }
  }
  template <class Fin> DI void run(f32x4 (&acc)[7][4], Ring r, Fin fin) const {
    auto none = [](int) {};
    Frag f0, f1;
    {
      Half h;
#pragma unroll
      for (int i = 0; i < 15; ++i) rd(f0, h, i, r.b0, r.w0, 0);
    }
#pragma unroll 1
    for (unsigned v = 0; v < 15; ++v) {
      LGKM(0);
      half<true>(acc, f0, f1, r.b0, r.w0, 1, none);
      LGKM(0); rawbar(); SB();
      half<true>(acc, f1, f0, r.b1, r.w1, 0, none);
      if (ISQ && HSLP) __builtin_amdgcn_s_sleep(HSLP);
      r.rotate();
    }
    LGKM(0);
    half<true>(acc, f0, f1, r.b0, r.w0, 1, none);
    LGKM(0); rawbar(); SB();
    half<false>(acc, f1, f0, 0, 0, 0, fin);
#pragma unroll
    for (int m = 22; m < 28; ++m) fin(m);
  }
};
template <bool ISQ> struct GemmLoad {
  static constexpr unsigned WROW = ISQ ? 8192 : 2048;
  rsrc_t srdB, srdW;
  unsigned voffW, vB0, lds0, lw, hh, p;
  DI GemmLoad(rsrc_t b, rsrc_t w, unsigned lds0_, unsigned lw_, unsigned hh_, unsigned p_) : srdB(b), srdW(w), lds0(lds0_), lw(lw_), hh(hh_), p(p_) {
    const unsigned lane = threadIdx.x & 63, l4 = lane >> 4, l15 = lane & 15;
    voffW = l4 * WROW + ((l15 ^ (ISQ ? (l4 << 2) | (lw & 2) : (((lw & 1) << 2) | l4) << 1)) << 4);
    vB0 = ISQ ? (lane >> 3) * 2048 + ((((lane & 7) ^ l4) << 4) ^ ((lw & 1) << 6)) : lane * 16;
  }
  DI unsigned kk(unsigned v) const {
    if (ISQ) return (v & 8) | ((v + hh + 2 * p) & 7);
    unsigned j = v + ((7 * p) >> 1);
    j = j >= 16 ? j - 14 : j;
    return (2 * hh + (v < 2 ? v : j)) & 15;
  }
  DI void w(unsigned v, unsigned slot) const {
#pragma unroll
    for (unsigned i = 0; i < 4; ++i) dma16<0>(lds0 + slot + lw * 1024, i * 4096, voffW, srdW, (kk(v) * 64 + 4 * lw) * WROW, 16 * i * WROW);
  }
  DI void b(unsigned v, unsigned slot) const {
    if constexpr (ISQ) {
#pragma unroll
      for (unsigned i = 0; i < 7; ++i) dma16<0>(lds0 + slot + lw * 1024, i * 4096, vB0, srdB, lw * 16384 + kk(v) * 128, i * 65536);
    } else {
#pragma unroll
      for (unsigned i = 0; i < 3; ++i) dma16<2>(lds0 + slot + lw * 1024, i * 4096, vB0, srdB, kk(v) * 14336 + lw * 1024, i * 4096);
      const unsigned x = lw < 2 ? 12288 + lw * 1024 : 14336 + 12288 + (3 - lw) * 1024;
      dma16<2>(lds0 + slot + x, 0, vB0, srdB, kk(v) * 14336 + x + (lw < 2 ? 0 : 32 * 28672 - 14336), 0);
#pragma unroll
      for (unsigned i = 0; i < 3; ++i) dma16<2>(lds0 + slot + (3 - lw) * 1024, 14336 + i * 4096, vB0, srdB, kk(v) * 14336 + (3 - lw) * 1024, 32 * 28672 + i * 4096);
    }
  }
  DI void tile(unsigned v, unsigned bs, unsigned ws) const { w(v, ws); b(v, bs); }
};

struct Score { f32x4 s[2][2]; };
struct Attn {
  f32x4 o[8][2], ls[2], negm[2];
  Score sa, sb;
  bf16x8 qf[4][2], kf[8], vf[8], ones;
  bf16x4 vh;
  float mref[2];
  unsigned ka[4], va[8];
  DI void rd_k(int f, int ofs) { kf[f] = lds128(ka[f >> 1] + ofs + (f & 1) * 4096); }
  DI void rd_v(int i, int ofs) { const bf16x4 x = ldstr(va[i >> 1] + ofs + (i & 1) * 4096); if (i & 1) vf[i >> 1] = cat(vh, x); else vh = x; }
  DI void qk(Score& n, int i) {
    const int f = i >> 1, b = i & 1;
    if (f < 2) asm("v_mfma_f32_16x16x32_bf16 %0, %1, %2, %3" : "=&v"(n.s[f][b]) : "v"(kf[f]), "v"(qf[0][b]), "v"(negm[b]));
    else mac16(n.s[f & 1][b], kf[f], qf[f >> 1][b]);
    if (i == 7) asm volatile("" :: "v"(negm[0]), "v"(negm[1]));
  }
  DI void settle() {
    asm volatile("s_nop 7" : "+v"(o[0][0]), "+v"(o[0][1]), "+v"(o[1][0]), "+v"(o[1][1]), "+v"(o[2][0]), "+v"(o[2][1]), "+v"(o[3][0]), "+v"(o[3][1]), "+v"(o[4][0]), "+v"(o[4][1]),
                 "+v"(o[5][0]), "+v"(o[5][1]), "+v"(o[6][0]), "+v"(o[6][1]), "+v"(o[7][0]), "+v"(o[7][1]), "+v"(ls[0]), "+v"(ls[1]));
  }
  template <bool FIRST> DI void rescale(Score& n, bool grow, bool pad) {
    if (FIRST || __builtin_expect(grow, 0)) {
      if constexpr (!FIRST) settle();
#pragma unroll
      for (int b = 0; b < 2; ++b) {
        const f32x4 &x = n.s[0][b], &y = n.s[1][b];
        const float mx = xrow_max(max2f(max3f(max3f(x[0], x[1], x[2]), x[3], y[0]), max3f(y[1], y[2], y[3])));
        const float d = FIRST ? ((b && pad) ? 1e30f : mx) : max2f(mx, 0.f);
        mref[b] += d;
        n.s[0][b] -= d; n.s[1][b] -= d;
        negm[b] = f32x4{-mref[b], -mref[b], -mref[b], -mref[b]};
        if constexpr (!FIRST) {
          const float f = __builtin_amdgcn_exp2f(-d);
          ls[b] *= f;
#pragma unroll
          for (int c = 0; c < 8; ++c) o[c][b] *= f;
        }
      }
      asm volatile("s_nop 1" : "+v"(negm[0]), "+v"(negm[1]));
    }
  }
  DI void advance(int delta) {
#pragma unroll
    for (int i = 0; i < 4; ++i) ka[i] += delta;
#pragma unroll
    for (int i = 0; i < 8; ++i) va[i] += delta;
  }
  DI void init(unsigned lds0, unsigned w4, unsigned qimg, unsigned kslot, unsigned slot0) {
    const unsigned lane = threadIdx.x & 63, l15 = lane & 15, l4 = lane >> 4, q4 = (lane >> 2) & 3, pp = lane & 3;
    const unsigned kb = l15 * 256 + ((l4 ^ l15) << 4);
    const unsigned vb = (4 * l4 + q4) * 256 + ((4 * (l4 & 1) + q4) << 5) + pp * 8;
    const bool pad = w4 == 3;
#pragma unroll
    for (int i = 0; i < 4; ++i) {
      ka[i] = lds0 + kslot + 16384 + (kb ^ (i << 6));
      const unsigned a = lds0 + qimg + w4 * 8192 + (kb ^ (i << 6));
      qf[i][0] = lds128(a); qf[i][1] = lds128(a + (pad ? 0 : 4096));
      rd_k(2 * i, 0); rd_k(2 * i + 1, 0);
      SB();
    }
#pragma unroll
    for (int c = 0; c < 8; ++c) { o[c][0] = f32x4{}; o[c][1] = f32x4{}; va[c] = lds0 + kslot + (vb ^ (c << 5)); }
    if (pad) {
#pragma unroll
      for (int i = 0; i < 4; ++i) qf[i][1] = bf16x8{};
    }
    ones = __builtin_bit_cast(bf16x8, u32x4{0x3f803f80u, 0x3f803f80u, 0x3f803f80u, 0x3f803f80u});
    ls[0] = ls[1] = negm[0] = negm[1] = f32x4{}; mref[0] = mref[1] = 0.f;
    SB();
    asm volatile("s_nop 1" : "+v"(negm[0]), "+v"(negm[1]), "+v"(ones), "+v"(qf[0][1]));
#pragma unroll
    for (int i = 0; i < 16; ++i) { qk(sa, i); SB(); }
#pragma unroll
    for (int f = 0; f < 8; ++f) rd_k(f, 8192);
    asm volatile("s_nop 7" : "+v"(sa.s[0][0]), "+v"(sa.s[0][1]), "+v"(sa.s[1][0]), "+v"(sa.s[1][1]));
    rescale<true>(sa, true, pad);
    advance(slot0 - kslot);
    SB();
  }
  template <bool NEXT, bool RDK, bool ADV> DI void sub(Score& c, Score& n, int ofs, int delta) {
    u32x4 p[2];
    LGKM(0);
#pragma unroll
    for (int i = 0; i < 16; ++i) {
      if constexpr (NEXT) { qk(n, i); SB(); }
      if (i & 1) { rd_v(i >> 1, ofs); SB(); }
      f32x4& x = c.s[(i >> 2) & 1][i >> 3];
      x[i & 3] = __builtin_amdgcn_exp2f(x[i & 3]);
      if (i >= 2 && !(i & 1)) { const int k = i - 2; const f32x4& y = c.s[(k >> 2) & 1][k >> 3]; p[k >> 3][(k >> 1) & 3] = cvtpk(y[k & 3], y[(k & 3) + 1]); }
      SB();
    }
    float m0, m1;
    bool grow = false;
#pragma unroll
    for (int i = 0; i < 18; ++i) {
      if (i == 0) { LGKM(4); p[1][3] = cvtpk(c.s[1][1][2], c.s[1][1][3]); SB(); }
      if (i == 4) LGKM(RDK ? 5 : 4);
      if (RDK && i == 8) LGKM(8);
      if (RDK && i == 12) LGKM(4);
      if (i < 16) mac16(o[i >> 1][i & 1], vf[i >> 1], __builtin_bit_cast(bf16x8, p[i & 1]));
      else mac16(ls[i & 1], ones, __builtin_bit_cast(bf16x8, p[i & 1]));
      SB();
      if (i < 8) rd_v(8 + i, ofs);
      if constexpr (RDK) { if (i >= 3 && i < 11) rd_k(i - 3, ofs); }
      if constexpr (NEXT) {
        if (i == 1) m0 = max3f(n.s[0][0][0], n.s[0][0][1], n.s[0][0][2]);
        if (i == 2) m1 = max3f(n.s[0][0][3], n.s[0][1][0], n.s[0][1][1]);
        if (i == 8) m0 = max3f(m0, n.s[0][1][2], n.s[0][1][3]);
        if (i == 9) m1 = max3f(m1, n.s[1][0][0], n.s[1][0][1]);
        if (i == 10) m0 = max3f(m0, n.s[1][0][2], n.s[1][0][3]);
        if (i == 11) { m1 = max3f(m1, n.s[1][1][0], n.s[1][1][1]); m0 = max3f(m0, n.s[1][1][2], n.s[1][1][3]); }
        if (i == 12) m0 = max2f(m0, m1);
        if (i == 13) grow = __any(m0 > THR);
      }
      if constexpr (ADV) {
        if (i >= 12 && i < 16) { vadd(va[2 * i - 24], delta); vadd(va[2 * i - 23], delta); }
        if (i >= 16) { vadd(ka[2 * i - 32], delta); vadd(ka[2 * i - 31], delta); }
      }
      SB();
    }
    if constexpr (NEXT) rescale<false>(n, grow, false);
  }
  template <int SLP> DI void run() {
    unsigned s0 = KVA, s1 = KVB, s2 = KVC, s3 = KVD;
    wait_bar<8>();
#pragma unroll 1
    for (unsigned n = 0; n < 15; ++n) {
      sub<true, true, false>(sa, sb, 0, 0);
      sub<true, true, true>(sb, sa, 8192, s1 - s0);
      if (SLP) __builtin_amdgcn_s_sleep(SLP);
      const unsigned t = s0; s0 = s1; s1 = s2; s2 = s3; s3 = t;
      wait_bar<0>();
    }
    sub<true, false, false>(sa, sb, 0, 0);
    sub<false, false, false>(sb, sa, 8192, 0);
    settle();
  }
  DI void finish(u32x4 (&of)[8]) {
#pragma unroll
    for (int b = 0; b < 2; ++b) {
      const float inv = 1.f / ls[b][0];
#pragma unroll
      for (int s = 0; s < 4; ++s) {
        const f32x4 x = o[2 * s][b] * inv, y = o[2 * s + 1][b] * inv;
        of[2 * s + b] = u32x4{cvtpk(x[0], x[1]), cvtpk(x[2], x[3]), cvtpk(y[0], y[1]), cvtpk(y[2], y[3])};
      }
    }
  }
};
DI void publish_o(const u32x4 (&of)[8], rsrc_t srdO, unsigned so, unsigned w4) {
  const unsigned vo = so + 2 * w4 * 1024 + (threadIdx.x & 63) * 16, vo1 = w4 == 3 ? 0x80000000u : vo + 1024;
#pragma unroll
  for (int s = 0; s < 4; ++s) {
    __builtin_amdgcn_raw_buffer_store_b128(of[2 * s], srdO, vo + s * 7168, 0, 16);
    __builtin_amdgcn_raw_buffer_store_b128(of[2 * s + 1], srdO, vo1 + s * 7168, 0, 16);
  }
}
template <int T> DI void put_o(const u32x4 (&of)[8], unsigned base, unsigned w4) {
  const unsigned a = base + 2 * w4 * 1024 + (threadIdx.x & 63) * 16;
  *(LAS u32x4*)(size_t)a = of[4 * T]; *(LAS u32x4*)(size_t)(a + 7168) = of[4 * T + 2];
  if (w4 < 3) { *(LAS u32x4*)(size_t)(a + 1024) = of[4 * T + 1]; *(LAS u32x4*)(size_t)(a + 8192) = of[4 * T + 3]; }
}

constexpr int NR = NRT;
constexpr int RT[6] = RTILES;
constexpr int RB[6] = RBURST;
constexpr unsigned mask_of(const int (&a)[6]) { unsigned m = 0; for (int i = 0; i < NR; ++i) m |= 1u << a[i]; return m; }
constexpr unsigned RMASK = mask_of(RT), BMASK = mask_of(RB);

__global__ __launch_bounds__(512, 2)
void k_fused(const u16* __restrict__ xA, const u16* __restrict__ kA, const u16* __restrict__ vA, const u16* __restrict__ wqA, const u16* __restrict__ woA,
             const u16* __restrict__ xB, const u16* __restrict__ kB, const u16* __restrict__ vB, const u16* __restrict__ wqB, const u16* __restrict__ woB,
             char* __restrict__ ws, u16* __restrict__ out) {
  extern __shared__ __attribute__((aligned(16))) char smem[];
  u64 pc0; asm volatile("s_getpc_b64 %0" : "=s"(pc0));
  const unsigned lds0 = (unsigned)(size_t)(LAS char*)smem;
  const unsigned lane = threadIdx.x & 63, wid = __builtin_amdgcn_readfirstlane(threadIdx.x >> 6), w4 = wid & 3;
  const unsigned bid = blockIdx.x, xcd = bid & 7, rr = bid >> 3, br = xcd & 1, g = xcd >> 1, hh = rr & 7, p = rr >> 3;
  const unsigned head = 8 * g + hh;
#define KROT 0
  const unsigned krot = KROT;
  STAMP(0, 0); CSTAMP(0, 4);
  const unsigned t0 = (unsigned)wall_clock64() + PHI;
  int tgate = 0;
  auto not_before = [&](int ticks, int gap) {
    ticks = ticks > tgate + gap ? ticks : tgate + gap;
    while ((tgate = (int)((unsigned)wall_clock64() - t0)) < ticks) __builtin_amdgcn_s_sleep(1);
  };
  const char* const kptr = (const char*)(br ? kB : kA) + (size_t)(2 * p * NH + head) * (SK * DH * 2);
  const char* const vptr = (const char*)(br ? vB : vA) + (size_t)(2 * p * NH + head) * (SK * DH * 2);
  const rsrc_t srdX = mkrsrc((const char*)(br ? xB : xA) + (size_t)p * (2 * SQ * DM * 2), 2 * SQ * DM * 2);
  const rsrc_t srdWq = mkrsrc((const char*)(br ? wqB : wqA) + head * 256, DM * NH * DH * 2 - head * 256);
  const rsrc_t srdWo = mkrsrc((const char*)(br ? woB : woA) + (size_t)g * (1024 * 2048) + hh * 256, 1024 * 2048 - hh * 256);
  const rsrc_t srdO = mkrsrc(ws + WS_O + (size_t)((br * 8 + 2 * p) * 32 + 8 * g) * 28672, (32 + 8) * 28672);
  const rsrc_t srdE = mkrsrc(ws + WS_X, 256 * 65536);
  char* const epoch = ws + WS_E + (br * 4 + p) * 64;
  char* const fam = ws + WS_F + ((br * 4 + g) * 8 + 2 * p) * 2048;
  u16* const outp = out + (size_t)((2 * p + (w4 >> 1)) * SQ) * 2048 + br * 1024 + hh * 128 + (w4 & 1) * 64;
  asm volatile("" :: "s"(kptr), "s"(vptr), "s"(srdX), "s"(srdWq), "s"(srdWo), "s"(srdO), "s"(srdE), "s"(outp));
  u64 tag;
  auto load_tag = [&]() { asm volatile("s_load_dwordx2 %0, %1, 0x0\n\ts_waitcnt lgkmcnt(0)" : "=&s"(tag) : "s"(epoch) : "memory"); tag += 1; };
  const unsigned l4 = lane >> 4, l15 = lane & 15;
  const unsigned voffV = l4 * 256 + ((l15 ^ ((((w4 & 1) << 2) | l4) << 1)) << 4), voffK = l4 * 256 + ((l15 ^ ((w4 << 2) | l4)) << 4);
  constexpr unsigned KVBYTES = NH * SK * DH * 2 + SK * DH * 2;
  const rsrc_t srdV = mkrsrc(vptr, KVBYTES);
  auto so_k = [&](int n) { return ((n + 1) >> 4) * (NH * SK * DH * 2) + ((n + 1 + krot) & 15) * 16384 + w4 * 1024; };
  auto so_v = [&](int n) { return (n >> 4) * (NH * SK * DH * 2) + ((n + krot) & 15) * 16384 + w4 * 1024; };
  auto issue_k = [&](int n, unsigned slot) {
    const rsrc_t r = mkrsrc(kptr, n < 31 ? KVBYTES : 0);
#pragma unroll
    for (unsigned i = 0; i < 4; ++i) dma16<1>(lds0 + slot + w4 * 1024, 16384 + i * 4096, voffK, r, so_k(n), i * 4096);
  };
  auto issue_kv = [&](int n, unsigned slot) {
    issue_k(n, slot);
#pragma unroll
    for (unsigned i = 0; i < 4; ++i) dma16<1>(lds0 + slot + w4 * 1024, i * 4096, voffV, srdV, so_v(n), i * 4096);
  };
  auto issue_kv_m = [&](auto NO, int n, unsigned slot) {
    constexpr int N = decltype(NO)::value;
    const rsrc_t r = mkrsrc(kptr, n < 31 ? KVBYTES : 0);
#pragma unroll
    for (unsigned i = 0; i < 4; ++i) { if (i % MGR == 0) wait_vm<N - MGR>(); dma16<1>(lds0 + slot + w4 * 1024, 16384 + i * 4096, voffK, r, so_k(n), i * 4096); }
#pragma unroll
    for (unsigned i = 0; i < 4; ++i) { if (i % MGR == 0) wait_vm<N - MGR>(); dma16<1>(lds0 + slot + w4 * 1024, i * 4096, voffV, srdV, so_v(n), i * 4096); }
  };
  const unsigned bid0 = bid & ~6u;

  if (wid < 4) {
    Attn at;
    {
      const char* a = (const char*)(pc0 & ~255ull) + (rr * 4 + w4) * 256 + (lane & 1) * 128;
      unsigned x;
      if (lane < 2) { asm volatile("global_load_dword %0, %1, off\n\ts_waitcnt vmcnt(0)" : "=v"(x) : "v"(a) : "memory"); }
    }
    load_tag();
    {
      f32x4 acc[7][4] = {};
      const Gemm<true> gm(lds0, w4);
      rawbar(); rawbar();
      const unsigned qa = lds0 + ((w4 >> 1) ? QI2 : QI1) + l15 * 256 + (l4 & 1) * 8 + (((8 * (w4 & 1) + (l4 >> 1)) ^ l15) << 4);
      gm.run(acc, QRING, [&](int m) {
        const f32x4 x = acc[m >> 2][m & 3] * QSCALE;
        *(LAS u32x2*)(size_t)((qa ^ ((m & 3) << 5)) + (m >> 2) * 4096) = u32x2{cvtpk(x[0], x[1]), cvtpk(x[2], x[3])};
      });
    }
    bar();
    STAMP(0, 1); CSTAMP(0, 5);
    at.init(lds0, w4, QI1, KVD, KVA);
    at.template run<SLP1>();
    STAMP(0, 2); CSTAMP(0, 6);
    const GemmLoad<false> gl(srdO, srdWo, lds0, w4, hh, p);
    bar();
    issue_kv(18, KVC);
    u32x4 of[8];
    at.finish(of);
    {
      unsigned ks = KVD, ks1 = KVA, ks2 = KVB, ks3 = KVC;
#pragma unroll 1
      for (unsigned n = 17; n < 30; ++n) {
        wait_vm<8>();
        not_before(PA2 + (n - 16) * PB2, n < GEND ? GAP2 : 0);
        bar();
        issue_kv_m(IC(NOUT2), n + 2, ks);
        const unsigned t = ks; ks = ks1; ks1 = ks2; ks2 = ks3; ks3 = t;
      }
    }
    gl.w(0, ORING.w0);
    put_o<0>(of, lds0 + ORING.b0, w4);
    wait_bar<12>();
    put_o<1>(of, lds0 + ORING.b1, w4);
    publish_o(of, srdO, hh * 28672, w4);
    wait_bar<12>();
    gl.w(1, ORING.w1); gl.w(2, ORING.w2);
    wait_vm<8>();
    if (lane == 0) st_flag(fam + (hh * 4 + w4) * 64, tag);
    f32x4 acc[7][4] = {};
    const Gemm<false> gm(lds0, w4);
    rawbar();
    STAMP(1, 0);
    wait_bar<0>();
    STAMP(1, 2);
    const unsigned xo = bid0 * 65536 + g * 16384 + w4 * 4096 + lane * 16;
    gm.run(acc, ORING, [&](int m) {
      const int j = m >> 2, n = m & 3;
      if ((n & 1) && (unsigned)(j >> 1) != g) {
        const f32x4 a = acc[j][n - 1], b = acc[j][n];
        const u32x4 w = {cvtpk(a[0], a[1]), cvtpk(a[2], a[3]), cvtpk(b[0], b[1]), cvtpk(b[2], b[3])};
        __builtin_amdgcn_raw_buffer_store_b128(w, srdE, xo + (j >> 1) * 131072 + ((j & 1) * 2 + (n >> 1)) * 1024, 0, 16);
      }
    });
    STAMP(1, 3);
    f32x4 t[2][4];
#pragma unroll
    for (int n = 0; n < 4; ++n) {
      t[0][n] = g == 0 ? acc[0][n] : g == 1 ? acc[2][n] : g == 2 ? acc[4][n] : acc[6][n];
      t[1][n] = g == 0 ? acc[1][n] : g == 1 ? acc[3][n] : acc[5][n];
    }
    wait_vm<0>();
    STAMP(1, 4);
    if (lane < 4) st_flag(ws + WS_G + (((bid0 + 2 * lane) * 4 + w4) * 4 + g) * 64, tag);
    poll(ws + WS_G + ((bid * 4 + w4) * 4 + (lane & 3)) * 64, lane < 4, tag);
    u32x4 e[3][2][2];
#pragma unroll
    for (int jj = 0; jj < 2; ++jj)
#pragma unroll
      for (int n = 0; n < 2; ++n)
#pragma unroll
        for (unsigned sr = 0; sr < 3; ++sr)
          e[sr][jj][n] = __builtin_amdgcn_raw_buffer_load_b128(srdE, (jj && g == 3) ? 0x80000000u : lane * 16 + (jj * 2 + n) * 1024, bid * 65536 + ((g + sr + 1) & 3) * 16384 + w4 * 4096, 16);
#pragma unroll
    for (int jj = 0; jj < 2; ++jj) {
      u16* o = outp + (size_t)((2 * g + jj) * 16 + l15) * 2048 + l4 * 4;
#pragma unroll
      for (int n = 0; n < 4; ++n) {
        f32x4 v = t[jj][n];
#pragma unroll
        for (int sr = 0; sr < 3; ++sr) {
          const unsigned x = e[sr][jj][n >> 1][2 * (n & 1)], y = e[sr][jj][n >> 1][2 * (n & 1) + 1];
          v += f32x4{__uint_as_float(x << 16), __uint_as_float(x & 0xffff0000u), __uint_as_float(y << 16), __uint_as_float(y & 0xffff0000u)};
        }
        u32x2 w = {cvtpk(v[0], v[1]), cvtpk(v[2], v[3])};
        if (!(jj && g == 3)) *(GAS u32x2*)(o + n * 16) = w;
      }
    }
    if (g == 0 && hh == 0 && wid == 0 && lane == 0) st_flag(epoch, tag);
    STAMP(1, 5); STAMP(1, 6); STAMP(1, 7); STAMP(0, 3);
  } else {
    Attn at;
    {
      u32x4 rk[6][8];
      const rsrc_t srdK = mkrsrc(kptr, KVBYTES);
      auto fetch = [&](auto RI) {
        constexpr int r = decltype(RI)::value;
        if constexpr (r < NR) {
#pragma unroll
          for (int i = 0; i < 4; ++i) ld16nt(rk[r][4 + i], voffK, srdK, so_k(RT[r]), i * 4096);
#pragma unroll
          for (int i = 0; i < 4; ++i) ld16nt(rk[r][i], voffV, srdV, so_v(RT[r]), i * 4096);
        }
      };
      auto put = [&](auto RI, unsigned slot) {
        constexpr int r = decltype(RI)::value;
        if constexpr (r < NR) {
#pragma unroll
          for (int i = 0; i < 8; ++i) *(LAS u32x4*)(size_t)(lds0 + slot + w4 * 1024 + lane * 16 + i * 4096) = rk[r][i];
        }
      };
      const GemmLoad<true> gl(srdX, srdWq, lds0, w4, hh, p);
      auto qload = [&](auto PHASE) {
        constexpr int PH = decltype(PHASE)::value;
        Ring r = QRING;
        gl.tile(0, r.b0, r.w0); rawbar();
        gl.tile(1, r.b1, r.w1);
        wait_bar<11>();
        gl.tile(2, r.b2, r.w2);
#pragma unroll
        for (int v = 0; v < 13; ++v) {
          constexpr unsigned BM = BMASK << (2 + PH);
          const int nb = ((BM >> v) & 1) + ((BM >> (v + 1)) & 1);
          if (nb == 0) wait_bar<11>(); else if (nb == 1) wait_bar<19>(); else wait_bar<27>();
          gl.tile(v + 3, r.b0, r.w0);
          if (v == RB[0] + PH) fetch(IC(0)); if (v == RB[1] + PH) fetch(IC(1)); if (v == RB[2] + PH) fetch(IC(2));
          if (v == RB[3] + PH) fetch(IC(3)); if (v == RB[4] + PH) fetch(IC(4)); if (v == RB[5] + PH) fetch(IC(5));
          r.rotate();
        }
        issue_k(-1, KVD);
        if constexpr (((BMASK << PH) >> 12) & 1) wait_bar<23>(); else wait_bar<15>();
        issue_kv(0, KVA);
        wait_bar<12>();
#pragma unroll
        for (int r = 0; r < NR; ++r) landed(rk[r]);
      };
#define STAG 1
      if (hh & 1) qload(IC(STAG)); else qload(IC(0));
      rawbar();
      issue_kv(1, KVB);
      wait_bar<16>();
      STAMP(0, 1); CSTAMP(0, 5);
      wait_bar<8>();
      put(IC(0), KVC);
      unsigned ks = KVD, ks1 = KVA, ks2 = KVB, ks3 = KVC;
#pragma unroll
      for (int n = 0; n < 15; ++n) {
        const int u = n + 3;
        if ((RMASK >> u) & 1) {
          if (u == RT[1]) put(IC(1), ks); else if (u == RT[2]) put(IC(2), ks); else if (u == RT[3]) put(IC(3), ks);
          else if (u == RT[4]) put(IC(4), ks); else put(IC(5), ks);
        } else issue_kv_m(IC(NOUT1), u, ks);
        const unsigned t = ks; ks = ks1; ks1 = ks2; ks2 = ks3; ks3 = t;
        const int nd = 2 - ((RMASK >> (n + 2)) & 1) - ((RMASK >> u) & 1);
        if (nd == 2) wait_vm<16>(); else if (nd == 1) wait_vm<8>(); else wait_vm<0>();
        not_before(PA1 + (n + 1) * PB1, GAP1);
        bar();
      }
    }
    load_tag();
    at.init(lds0, w4, QI2, KVD, KVA);
    STAMP(0, 2); CSTAMP(0, 6);
    at.template run<0>();
    STAMP(1, 0);
    bar();
    u32x4 of[8];
    at.finish(of);
    publish_o(of, srdO, (32 + hh) * 28672, w4);
    put_o<0>(of, lds0 + ORING.b0 + 14336, w4); put_o<1>(of, lds0 + ORING.b1 + 14336, w4);
    const GemmLoad<false> gl(srdO, srdWo, lds0, w4, hh, p);
    bar();
    wait_vm<0>();
    if (lane == 0) st_flag(fam + 2048 + (hh * 4 + w4) * 64, tag);
    rawbar();
    Ring r = ORING;
    gl.w(3, r.w0);
    STAMP(1, 1);
    poll(fam + lane * 64, true, tag);
    STAMP(1, 2);
    gl.b(2, r.b2); gl.b(3, r.b0);
    wait_bar<7>();
    r.rotate();
    gl.tile(4, r.b0, r.w0);
    r.rotate();
#pragma unroll 1
    for (unsigned v = 2; v < 13; ++v) { wait_bar<11>(); gl.tile(v + 3, r.b0, r.w0); r.rotate(); }
    wait_bar<11>();
    wait_bar<0>();
    rawbar();
    STAMP(1, 3); STAMP(1, 4);
    STAMP(1, 5); STAMP(1, 6); STAMP(1, 7); STAMP(0, 3);
  }
}

extern "C" void kernel_launch(void* const* d_in, const int* in_sizes, int n_in, void* d_out, int out_size, void* d_ws, size_t ws_size, hipStream_t stream) {
  (void)hipFuncSetAttribute((const void*)k_fused, hipFuncAttributeMaxDynamicSharedMemorySize, 163840);
  k_fused<<<256, 512, 163840, stream>>>((const u16*)d_in[0], (const u16*)d_in[1], (const u16*)d_in[2], (const u16*)d_in[3], (const u16*)d_in[4],
                                        (const u16*)d_in[5], (const u16*)d_in[6], (const u16*)d_in[7], (const u16*)d_in[8], (const u16*)d_in[9],
                                        (char*)d_ws, (u16*)d_out);
}
```

```cpp
#include <hip/hip_runtime.h>
#include <stdint.h>
#include <type_traits>

typedef unsigned short u16;
typedef unsigned long long u64;
typedef __bf16 bf16x8 __attribute__((ext_vector_type(8)));
typedef __bf16 bf16x4 __attribute__((ext_vector_type(4)));
typedef __bf16 bf16x2 __attribute__((ext_vector_type(2)));
typedef float f32x16 __attribute__((ext_vector_type(16)));
typedef float f32x4 __attribute__((ext_vector_type(4)));
typedef float f32x2 __attribute__((ext_vector_type(2)));
typedef unsigned u32x4 __attribute__((ext_vector_type(4)));
typedef unsigned u32x2 __attribute__((ext_vector_type(2)));
#define LAS __attribute__((address_space(3)))
#define GAS __attribute__((address_space(1)))
#define DI __device__ __forceinline__
#define SB() __builtin_amdgcn_sched_barrier(0)
#define LGKM(n) do { __builtin_amdgcn_s_waitcnt(0xC07F | ((n) << 8)); SB(); } while (0)
#define IC(n) std::integral_constant<int, n>{}
typedef __amdgpu_buffer_rsrc_t rsrc_t;

constexpr int SQ = 112, DM = 1024, NH = 32, SK = 1024, DH = 128;
constexpr size_t WS_O = 0;
constexpr size_t WS_X = (size_t)16 << 20;
constexpr size_t WS_F = (size_t)48 << 20;
constexpr size_t WS_G = WS_F + 131072;
constexpr size_t WS_E = WS_G + 524288;
constexpr size_t WS_ST = (size_t)64 << 20;
constexpr float QSCALE = 0.08838834764831845f * 1.4426950408889634f;
constexpr float THR = 8.0f;
constexpr unsigned SPIN = 1u << 18;

#define PB1 96
#define PA1 1680
#define PB2 111
#define PA2 3220
#define PHI (((rr * 5) & 31) * 3 - 96)
#define YAUX 0
#define WARM 2
#define SMOOTH 0
#define KROT 0
#define RDN 22
#define QW 8
#define NRT 3
#define RTILES {2, 7, 12, 0, 0, 0}
#define RBURST {3, 7, 10, 99, 99, 99}
#define CSTAMP(kern, idx) do {} while (0)
#define STAMP(kern, idx) do {} while (0)

DI rsrc_t mkrsrc(const void* p, unsigned bytes) { return __builtin_amdgcn_make_buffer_rsrc((void*)p, (short)0, (int)bytes, 0x00020000); }
template <int POL> DI void dma16(unsigned mb, unsigned mi, unsigned voff, rsrc_t srd, unsigned sb, unsigned si) {
  unsigned t;
  if constexpr (POL == 1)      asm volatile("s_add_u32 m0, %1, %2\n\ts_add_u32 %0, %5, %6\n\tbuffer_load_dwordx4 %3, %4, %0 offen nt lds"  : "=&s"(t) : "s"(mb), "n"(mi), "v"(voff), "s"(srd), "s"(sb), "n"(si) : "memory", "scc");
  else if constexpr (POL == 2) asm volatile("s_add_u32 m0, %1, %2\n\ts_add_u32 %0, %5, %6\n\tbuffer_load_dwordx4 %3, %4, %0 offen sc1 lds" : "=&s"(t) : "s"(mb), "n"(mi), "v"(voff), "s"(srd), "s"(sb), "n"(si) : "memory", "scc");
  else                         asm volatile("s_add_u32 m0, %1, %2\n\ts_add_u32 %0, %5, %6\n\tbuffer_load_dwordx4 %3, %4, %0 offen lds"     : "=&s"(t) : "s"(mb), "n"(mi), "v"(voff), "s"(srd), "s"(sb), "n"(si) : "memory", "scc");
}
DI void ld16nt(u32x4& d, unsigned voff, rsrc_t srd, unsigned sb, unsigned si) {
  unsigned t;
  asm volatile("s_add_u32 %1, %4, %5\n\tbuffer_load_dwordx4 %0, %2, %3, %1 offen nt" : "=v"(d), "=&s"(t) : "v"(voff), "s"(srd), "s"(sb), "n"(si) : "memory", "scc");
}
DI void landed(u32x4 (&r)[8]) { asm volatile("" : "+v"(r[0]), "+v"(r[1]), "+v"(r[2]), "+v"(r[3]), "+v"(r[4]), "+v"(r[5]), "+v"(r[6]), "+v"(r[7])); }
template <int N> DI void wait_bar() { asm volatile("s_waitcnt vmcnt(%0) lgkmcnt(0)\n\ts_barrier" ::"n"(N) : "memory"); }
template <int N> DI void wait_vm() { asm volatile("s_waitcnt vmcnt(%0)" ::"n"(N) : "memory"); }
DI void bar() { asm volatile("s_waitcnt lgkmcnt(0)\n\ts_barrier" ::: "memory"); }
DI void rawbar() { asm volatile("s_barrier" ::: "memory"); }
DI bf16x8 lds128(unsigned a) { return *(const LAS bf16x8*)(size_t)a; }
DI bf16x4 ldstr(unsigned a) { return __builtin_amdgcn_ds_read_tr16_b64_v4bf16((LAS bf16x4*)(size_t)a); }
DI bf16x8 cat(bf16x4 a, bf16x4 b) { return __builtin_shufflevector(a, b, 0, 1, 2, 3, 4, 5, 6, 7); }
DI f32x16 mfma32(bf16x8 a, bf16x8 b, f32x16 c) { return __builtin_amdgcn_mfma_f32_32x32x16_bf16(a, b, c, 0, 0, 0); }
DI f32x4 mfma16(bf16x8 a, bf16x8 b, f32x4 c) { return __builtin_amdgcn_mfma_f32_16x16x32_bf16(a, b, c, 0, 0, 0); }
DI void mac16(f32x4& c, bf16x8 a, bf16x8 b) { asm("v_mfma_f32_16x16x32_bf16 %0, %1, %2, %0" : "+v"(c) : "v"(a), "v"(b)); }
DI unsigned cvtpk(float lo, float hi) { f32x2 v = {lo, hi}; return __builtin_bit_cast(unsigned, __builtin_convertvector(v, bf16x2)); }
DI u32x4 pack8(const f32x16& a, int o, float s) {
  u32x4 w = {cvtpk(a[o] * s, a[o + 1] * s), cvtpk(a[o + 2] * s, a[o + 3] * s), cvtpk(a[o + 4] * s, a[o + 5] * s), cvtpk(a[o + 6] * s, a[o + 7] * s)};
  return w;
}
DI float fadd(float a, float b) { float r; asm("v_add_f32_e32 %0, %1, %2" : "=v"(r) : "v"(a), "v"(b)); return r; }
DI void vadd(unsigned& a, int d) { asm volatile("v_add_u32_e32 %0, %1, %0" : "+v"(a) : "s"(d)); }
DI float max2f(float a, float b) { return __builtin_elementwise_maximum(a, b); }
DI float max3f(float a, float b, float c) { return max2f(max2f(a, b), c); }
DI float xrow_max(float x) {
  auto a = __builtin_amdgcn_permlane16_swap(__float_as_uint(x), __float_as_uint(x), false, false);
  x = max2f(__uint_as_float(a[0]), __uint_as_float(a[1]));
  auto b = __builtin_amdgcn_permlane32_swap(__float_as_uint(x), __float_as_uint(x), false, false);
  return max2f(__uint_as_float(b[0]), __uint_as_float(b[1]));
}
DI float xhalf_max(float x) { auto r = __builtin_amdgcn_permlane32_swap(__float_as_uint(x), __float_as_uint(x), false, false); return max2f(__uint_as_float(r[0]), __uint_as_float(r[1])); }
DI float xhalf_sum(float x) { auto r = __builtin_amdgcn_permlane32_swap(__float_as_uint(x), __float_as_uint(x), false, false); return __uint_as_float(r[0]) + __uint_as_float(r[1]); }
DI void st_flag(char* p, u64 v) { __hip_atomic_store((GAS u64*)p, v, __ATOMIC_RELAXED, __HIP_MEMORY_SCOPE_AGENT); }
DI u64 ld_flag(const char* p) { return __hip_atomic_load((GAS u64*)p, __ATOMIC_RELAXED, __HIP_MEMORY_SCOPE_AGENT); }
DI void poll(const char* p, bool on, u64 tag) {
#pragma unroll 1
  for (unsigned spin = 0; spin < SPIN; ++spin) {
    const u64 f = on ? ld_flag(p) : tag;
    if (__all(f == tag)) break;
    __builtin_amdgcn_s_sleep(1);
  }
  asm volatile("" ::: "memory");
}

constexpr unsigned KVA = 0, KVB = 65536, KVC = 98304, KVD = 32768;
constexpr unsigned QI1 = 98304, QI2 = 131072;
struct Ring { unsigned b0, b1, b2, w0, w1, w2; DI void rotate() { const unsigned t = b0, u = w0; b0 = b1; b1 = b2; b2 = t; w0 = w1; w1 = w2; w2 = u; } };
constexpr Ring QRING = {118784, 0, 73728, 118784 + 28672, 28672, 73728 + 28672};
constexpr Ring ORING = {16384, 73728, 45056, 131072, 147456, 0};

template <bool ISQ> struct Gemm {
  struct Frag { bf16x8 a[4], b[7]; };
  unsigned abase, bbase;
  DI Gemm(unsigned lds0, unsigned cw) {
    const unsigned lane = threadIdx.x & 63, l4 = lane >> 4, l15 = lane & 15, q4 = (lane >> 2) & 3, pp = lane & 3;
    abase = lds0 + (ISQ ? 8 * l4 + q4 : 4 * l4 + q4) * 256 + (((4 * (cw & 1)) ^ (ISQ ? (q4 << 1) | (l4 & 1) : 4 * (l4 & 1) + q4)) << 5) + pp * 8;
    bbase = lds0 + (cw >> 1) * 14336 + (ISQ ? l15 * 128 + ((l4 ^ (l15 >> 1)) << 4) : lane * 16);
  }
  struct Half { bf16x4 a[8]; };
  DI void rd(Frag& f, Half& h, int i, unsigned bs, unsigned ws, int s) const {
    if (i < 8) { h.a[i] = ldstr(((abase + ws) ^ ((i >> 1) << 5)) + s * 8192 + (i & 1) * (ISQ ? 1024 : 4096)); if (i & 1) f.a[i >> 1] = cat(h.a[i - 1], h.a[i]); }
    else if (i < 15) { const int j = i - 8; f.b[j] = ISQ ? lds128(((bbase + bs) ^ (s << 6)) + j * 2048) : lds128(bbase + bs + s * 7168 + j * 1024); }
  }
  template <bool RD, class Fin> DI void half(f32x4 (&acc)[7][4], const Frag& c, Frag& n, unsigned bs, unsigned ws, int s, Fin fin) const {
    Half h;
#pragma unroll
    for (int m = 0; m < 28; ++m) {
      acc[m >> 2][m & 3] = mfma16(c.a[m & 3], c.b[m >> 2], acc[m >> 2][m & 3]);
      SB();
      if (m >= 6) { fin(m - 6); SB(); }
      if constexpr (RD) { if (m < RDN) { if (RDN == 15) rd(n, h, m, bs, ws, s); else if ((m % 3) != 2) rd(n, h, m - m / 3, bs, ws, s); } SB(); }
    }
  }
  template <class Fin> DI void run(f32x4 (&acc)[7][4], Ring r, Fin fin) const {
    auto none = [](int) {};
    Frag f0, f1;
    {
      Half h;
#pragma unroll
      for (int i = 0; i < 15; ++i) rd(f0, h, i, r.b0, r.w0, 0);
    }
#pragma unroll 1
    for (unsigned v = 0; v < 15; ++v) {
      LGKM(0);
      half<true>(acc, f0, f1, r.b0, r.w0, 1, none);
      LGKM(0); rawbar(); SB();
      half<true>(acc, f1, f0, r.b1, r.w1, 0, none);
      r.rotate();
    }
    LGKM(0);
    half<true>(acc, f0, f1, r.b0, r.w0, 1, none);
    LGKM(0); rawbar(); SB();
    half<false>(acc, f1, f0, 0, 0, 0, fin);
#pragma unroll
    for (int m = 22; m < 28; ++m) fin(m);
  }
};
template <bool ISQ> struct GemmLoad {
  static constexpr unsigned WROW = ISQ ? 8192 : 2048;
  rsrc_t srdB, srdW;
  unsigned voffW, vB0, lds0, lw, hh, p;
  DI GemmLoad(rsrc_t b, rsrc_t w, unsigned lds0_, unsigned lw_, unsigned hh_, unsigned p_) : srdB(b), srdW(w), lds0(lds0_), lw(lw_), hh(hh_), p(p_) {
    const unsigned lane = threadIdx.x & 63, l4 = lane >> 4, l15 = lane & 15;
    voffW = l4 * WROW + ((l15 ^ (ISQ ? (l4 << 2) | (lw & 2) : (((lw & 1) << 2) | l4) << 1)) << 4);
    vB0 = ISQ ? (lane >> 3) * 2048 + ((((lane & 7) ^ l4) << 4) ^ ((lw & 1) << 6)) : lane * 16;
  }
  DI unsigned kk(unsigned v) const {
    if (ISQ) return (v & 8) | ((v + hh + 2 * p) & 7);
    unsigned j = v + ((7 * p) >> 1);
    j = j >= 16 ? j - 14 : j;
    return (2 * hh + (v < 2 ? v : j)) & 15;
  }
  DI void w(unsigned v, unsigned slot) const {
#pragma unroll
    for (unsigned i = 0; i < 4; ++i) dma16<0>(lds0 + slot + lw * 1024, i * 4096, voffW, srdW, (kk(v) * 64 + 4 * lw) * WROW, 16 * i * WROW);
  }
  DI void b(unsigned v, unsigned slot) const {
    if constexpr (ISQ) {
#pragma unroll
      for (unsigned i = 0; i < 7; ++i) dma16<0>(lds0 + slot + lw * 1024, i * 4096, vB0, srdB, lw * 16384 + kk(v) * 128, i * 65536);
    } else {
#pragma unroll
      for (unsigned i = 0; i < 3; ++i) dma16<2>(lds0 + slot + lw * 1024, i * 4096, vB0, srdB, kk(v) * 14336 + lw * 1024, i * 4096);
      const unsigned x = lw < 2 ? 12288 + lw * 1024 : 14336 + 12288 + (3 - lw) * 1024;
      dma16<2>(lds0 + slot + x, 0, vB0, srdB, kk(v) * 14336 + x + (lw < 2 ? 0 : 32 * 28672 - 14336), 0);
#pragma unroll
      for (unsigned i = 0; i < 3; ++i) dma16<2>(lds0 + slot + (3 - lw) * 1024, 14336 + i * 4096, vB0, srdB, kk(v) * 14336 + (3 - lw) * 1024, 32 * 28672 + i * 4096);
    }
  }
  DI void tile(unsigned v, unsigned bs, unsigned ws) const { w(v, ws); b(v, bs); }
};

struct Score { f32x4 s[2][2]; };
struct Attn {
  f32x4 o[8][2], ls[2], negm[2];
  Score sa, sb;
  bf16x8 qf[4][2], kf[8], vf[8], ones;
  bf16x4 vh;
  float mref[2];
  unsigned ka[4], va[8];
  DI void rd_k(int f, int ofs) { kf[f] = lds128(ka[f >> 1] + ofs + (f & 1) * 4096); }
  DI void rd_v(int i, int ofs) { const bf16x4 x = ldstr(va[i >> 1] + ofs + (i & 1) * 4096); if (i & 1) vf[i >> 1] = cat(vh, x); else vh = x; }
  DI void qk(Score& n, int i) {
    const int f = i >> 1, b = i & 1;
    if (f < 2) asm("v_mfma_f32_16x16x32_bf16 %0, %1, %2, %3" : "=&v"(n.s[f][b]) : "v"(kf[f]), "v"(qf[0][b]), "v"(negm[b]));
    else mac16(n.s[f & 1][b], kf[f], qf[f >> 1][b]);
    if (i == 7) asm volatile("" :: "v"(negm[0]), "v"(negm[1]));
  }
  DI void settle() {
    asm volatile("s_nop 7" : "+v"(o[0][0]), "+v"(o[0][1]), "+v"(o[1][0]), "+v"(o[1][1]), "+v"(o[2][0]), "+v"(o[2][1]), "+v"(o[3][0]), "+v"(o[3][1]), "+v"(o[4][0]), "+v"(o[4][1]),
                 "+v"(o[5][0]), "+v"(o[5][1]), "+v"(o[6][0]), "+v"(o[6][1]), "+v"(o[7][0]), "+v"(o[7][1]), "+v"(ls[0]), "+v"(ls[1]));
  }
  template <bool FIRST> DI void rescale(Score& n, bool grow, bool pad) {
    if (FIRST || __builtin_expect(grow, 0)) {
      if constexpr (!FIRST) settle();
#pragma unroll
      for (int b = 0; b < 2; ++b) {
        const f32x4 &x = n.s[0][b], &y = n.s[1][b];
        const float mx = xrow_max(max2f(max3f(max3f(x[0], x[1], x[2]), x[3], y[0]), max3f(y[1], y[2], y[3])));
        const float d = FIRST ? ((b && pad) ? 1e30f : mx) : max2f(mx, 0.f);
        mref[b] += d;
        n.s[0][b] -= d; n.s[1][b] -= d;
        negm[b] = f32x4{-mref[b], -mref[b], -mref[b], -mref[b]};
        if constexpr (!FIRST) {
          const float f = __builtin_amdgcn_exp2f(-d);
          ls[b] *= f;
#pragma unroll
          for (int c = 0; c < 8; ++c) o[c][b] *= f;
        }
      }
      asm volatile("s_nop 1" : "+v"(negm[0]), "+v"(negm[1]));
    }
  }
  DI void advance(int delta) {
#pragma unroll
    for (int i = 0; i < 4; ++i) ka[i] += delta;
#pragma unroll
    for (int i = 0; i < 8; ++i) va[i] += delta;
  }
  DI void init(unsigned lds0, unsigned w4, unsigned qimg, unsigned kslot, unsigned slot0) {
    const unsigned lane = threadIdx.x & 63, l15 = lane & 15, l4 = lane >> 4, q4 = (lane >> 2) & 3, pp = lane & 3;
    const unsigned kb = l15 * 256 + ((l4 ^ l15) << 4);
    const unsigned vb = (4 * l4 + q4) * 256 + ((4 * (l4 & 1) + q4) << 5) + pp * 8;
    const bool pad = w4 == 3;
#pragma unroll
    for (int i = 0; i < 4; ++i) {
      ka[i] = lds0 + kslot + 16384 + (kb ^ (i << 6));
      const unsigned a = lds0 + qimg + w4 * 8192 + (kb ^ (i << 6));
      qf[i][0] = lds128(a); qf[i][1] = lds128(a + (pad ? 0 : 4096));
      rd_k(2 * i, 0); rd_k(2 * i + 1, 0);
      SB();
    }
#pragma unroll
    for (int c = 0; c < 8; ++c) { o[c][0] = f32x4{}; o[c][1] = f32x4{}; va[c] = lds0 + kslot + (vb ^ (c << 5)); }
    if (pad) {
#pragma unroll
      for (int i = 0; i < 4; ++i) qf[i][1] = bf16x8{};
    }
    ones = __builtin_bit_cast(bf16x8, u32x4{0x3f803f80u, 0x3f803f80u, 0x3f803f80u, 0x3f803f80u});
    ls[0] = ls[1] = negm[0] = negm[1] = f32x4{}; mref[0] = mref[1] = 0.f;
    SB();
    asm volatile("s_nop 1" : "+v"(negm[0]), "+v"(negm[1]), "+v"(ones), "+v"(qf[0][1]));
#pragma unroll
    for (int i = 0; i < 16; ++i) { qk(sa, i); SB(); }
#pragma unroll
    for (int f = 0; f < 8; ++f) rd_k(f, 8192);
    asm volatile("s_nop 7" : "+v"(sa.s[0][0]), "+v"(sa.s[0][1]), "+v"(sa.s[1][0]), "+v"(sa.s[1][1]));
    rescale<true>(sa, true, pad);
    advance(slot0 - kslot);
    SB();
  }
  template <bool NEXT, bool RDK, bool ADV> DI void sub(Score& c, Score& n, int ofs, int delta) {
    u32x4 p[2];
    LGKM(0);
#pragma unroll
    for (int i = 0; i < 16; ++i) {
      if constexpr (NEXT) { qk(n, i); SB(); }
      if (i & 1) { rd_v(i >> 1, ofs); SB(); }
      f32x4& x = c.s[(i >> 2) & 1][i >> 3];
      x[i & 3] = __builtin_amdgcn_exp2f(x[i & 3]);
      if (i >= 2 && !(i & 1)) { const int k = i - 2; const f32x4& y = c.s[(k >> 2) & 1][k >> 3]; p[k >> 3][(k >> 1) & 3] = cvtpk(y[k & 3], y[(k & 3) + 1]); }
      SB();
    }
    float m0, m1;
    bool grow = false;
#pragma unroll
    for (int i = 0; i < 18; ++i) {
      if (i == 0) { LGKM(4); p[1][3] = cvtpk(c.s[1][1][2], c.s[1][1][3]); SB(); }
      if (i == 4) LGKM(RDK ? 5 : 4);
      if (RDK && i == 8) LGKM(8);
      if (RDK && i == 12) LGKM(4);
      if (i < 16) mac16(o[i >> 1][i & 1], vf[i >> 1], __builtin_bit_cast(bf16x8, p[i & 1]));
      else mac16(ls[i & 1], ones, __builtin_bit_cast(bf16x8, p[i & 1]));
      SB();
      if (i < 8) rd_v(8 + i, ofs);
      if constexpr (RDK) { if (i >= 3 && i < 11) rd_k(i - 3, ofs); }
      if constexpr (NEXT) {
        if (i == 1) m0 = max3f(n.s[0][0][0], n.s[0][0][1], n.s[0][0][2]);
        if (i == 2) m1 = max3f(n.s[0][0][3], n.s[0][1][0], n.s[0][1][1]);
        if (i == 8) m0 = max3f(m0, n.s[0][1][2], n.s[0][1][3]);
        if (i == 9) m1 = max3f(m1, n.s[1][0][0], n.s[1][0][1]);
        if (i == 10) m0 = max3f(m0, n.s[1][0][2], n.s[1][0][3]);
        if (i == 11) { m1 = max3f(m1, n.s[1][1][0], n.s[1][1][1]); m0 = max3f(m0, n.s[1][1][2], n.s[1][1][3]); }
        if (i == 12) m0 = max2f(m0, m1);
        if (i == 13) grow = __any(m0 > THR);
      }
      if constexpr (ADV) {
        if (i >= 12 && i < 16) { vadd(va[2 * i - 24], delta); vadd(va[2 * i - 23], delta); }
        if (i >= 16) { vadd(ka[2 * i - 32], delta); vadd(ka[2 * i - 31], delta); }
      }
      SB();
    }
    if constexpr (NEXT) rescale<false>(n, grow, false);
  }
  DI void run() {
    unsigned s0 = KVA, s1 = KVB, s2 = KVC, s3 = KVD;
    wait_bar<8>();
#pragma unroll 1
    for (unsigned n = 0; n < 15; ++n) {
      sub<true, true, false>(sa, sb, 0, 0);
      sub<true, true, true>(sb, sa, 8192, s1 - s0);
      const unsigned t = s0; s0 = s1; s1 = s2; s2 = s3; s3 = t;
      wait_bar<0>();
    }
    sub<true, false, false>(sa, sb, 0, 0);
    sub<false, false, false>(sb, sa, 8192, 0);
    settle();
  }
  DI void finish(u32x4 (&of)[8]) {
#pragma unroll
    for (int b = 0; b < 2; ++b) {
      const float inv = 1.f / ls[b][0];
#pragma unroll
      for (int s = 0; s < 4; ++s) {
        const f32x4 x = o[2 * s][b] * inv, y = o[2 * s + 1][b] * inv;
        of[2 * s + b] = u32x4{cvtpk(x[0], x[1]), cvtpk(x[2], x[3]), cvtpk(y[0], y[1]), cvtpk(y[2], y[3])};
      }
    }
  }
};
DI void publish_o(const u32x4 (&of)[8], rsrc_t srdO, unsigned so, unsigned w4) {
  const unsigned vo = so + 2 * w4 * 1024 + (threadIdx.x & 63) * 16, vo1 = w4 == 3 ? 0x80000000u : vo + 1024;
#pragma unroll
  for (int s = 0; s < 4; ++s) {
    __builtin_amdgcn_raw_buffer_store_b128(of[2 * s], srdO, vo + s * 7168, 0, 16);
    __builtin_amdgcn_raw_buffer_store_b128(of[2 * s + 1], srdO, vo1 + s * 7168, 0, 16);
  }
}
template <int T> DI void put_o(const u32x4 (&of)[8], unsigned base, unsigned w4) {
  const unsigned a = base + 2 * w4 * 1024 + (threadIdx.x & 63) * 16;
  *(LAS u32x4*)(size_t)a = of[4 * T]; *(LAS u32x4*)(size_t)(a + 7168) = of[4 * T + 2];
  if (w4 < 3) { *(LAS u32x4*)(size_t)(a + 1024) = of[4 * T + 1]; *(LAS u32x4*)(size_t)(a + 8192) = of[4 * T + 3]; }
}

constexpr int NR = NRT;
constexpr int RT[6] = RTILES;
constexpr int RB[6] = RBURST;
constexpr unsigned mask_of(const int (&a)[6]) { unsigned m = 0; for (int i = 0; i < NR; ++i) m |= 1u << a[i]; return m; }
constexpr unsigned RMASK = mask_of(RT), BMASK = mask_of(RB);
constexpr int run_of(int u) { int k = 0; while (!((RMASK >> (u - 1 - k)) & 1)) ++k; return k; }

__global__ __launch_bounds__(512, 2)
void k_fused(const u16* __restrict__ xA, const u16* __restrict__ kA, const u16* __restrict__ vA, const u16* __restrict__ wqA, const u16* __restrict__ woA,
             const u16* __restrict__ xB, const u16* __restrict__ kB, const u16* __restrict__ vB, const u16* __restrict__ wqB, const u16* __restrict__ woB,
             char* __restrict__ ws, u16* __restrict__ out) {
  extern __shared__ __attribute__((aligned(16))) char smem[];
  const unsigned lds0 = (unsigned)(size_t)(LAS char*)smem;
  const unsigned lane = threadIdx.x & 63, wid = __builtin_amdgcn_readfirstlane(threadIdx.x >> 6), w4 = wid & 3;
  const unsigned bid = blockIdx.x, xcd = bid & 7, rr = bid >> 3, br = xcd & 1, g = xcd >> 1, hh = rr & 7, p = rr >> 3;
  const unsigned head = 8 * g + hh;
  const bool grpP = wid < 4;
  const unsigned krot = KROT;
  STAMP(0, 0); CSTAMP(0, 4);
  const unsigned t0 = (unsigned)wall_clock64() + PHI;
  auto not_before = [&](int ticks) { while ((int)((unsigned)wall_clock64() - t0) < ticks) __builtin_amdgcn_s_sleep(1); };
  const char* const kptr = (const char*)(br ? kB : kA) + (size_t)(2 * p * NH + head) * (SK * DH * 2);
  const char* const vptr = (const char*)(br ? vB : vA) + (size_t)(2 * p * NH + head) * (SK * DH * 2);
  const rsrc_t srdX = mkrsrc((const char*)(br ? xB : xA) + (size_t)p * (2 * SQ * DM * 2), 2 * SQ * DM * 2);
  const rsrc_t srdWq = mkrsrc((const char*)(br ? wqB : wqA) + head * 256, DM * NH * DH * 2 - head * 256);
  const rsrc_t srdWo = mkrsrc((const char*)(br ? woB : woA) + (size_t)g * (1024 * 2048) + hh * 256, 1024 * 2048 - hh * 256);
  const rsrc_t srdO = mkrsrc(ws + WS_O + (size_t)((br * 8 + 2 * p) * 32 + 8 * g) * 28672, (32 + 8) * 28672);
  const rsrc_t srdE = mkrsrc(ws + WS_X, 256 * 65536);
  const rsrc_t srdY = mkrsrc(out + (size_t)(2 * p * SQ) * 2048 + br * 1024 + hh * 128, 2 * SQ * 4096 - (br * 1024 + hh * 128) * 2);
  char* const epoch = ws + WS_E + (br * 4 + p) * 64;
  char* const fam = ws + WS_F + ((br * 4 + g) * 8 + 2 * p) * 2048;
  asm volatile("" :: "s"(kptr), "s"(vptr), "s"(srdX), "s"(srdWq), "s"(srdWo), "s"(srdO), "s"(srdE), "s"(srdY), "s"(t0));
  const unsigned l4 = lane >> 4, l15 = lane & 15;
  const unsigned voffV = l4 * 256 + ((l15 ^ ((((w4 & 1) << 2) | l4) << 1)) << 4), voffK = l4 * 256 + ((l15 ^ ((w4 << 2) | l4)) << 4);
  constexpr unsigned KVBYTES = NH * SK * DH * 2 + SK * DH * 2;
  const rsrc_t srdV = mkrsrc(vptr, KVBYTES);
  auto so_k = [&](int n) { return ((n + 1) >> 4) * (NH * SK * DH * 2) + ((n + 1 + krot) & 15) * 16384 + w4 * 1024; };
  auto so_v = [&](int n) { return (n >> 4) * (NH * SK * DH * 2) + ((n + krot) & 15) * 16384 + w4 * 1024; };
  auto issue_k = [&](int n, unsigned slot) {
    const rsrc_t r = mkrsrc(kptr, n < 31 ? KVBYTES : 0);
#pragma unroll
    for (unsigned i = 0; i < 4; ++i) dma16<1>(lds0 + slot + w4 * 1024, 16384 + i * 4096, voffK, r, so_k(n), i * 4096);
  };
  auto issue_kv = [&](int n, unsigned slot) {
    issue_k(n, slot);
#pragma unroll
    for (unsigned i = 0; i < 4; ++i) dma16<1>(lds0 + slot + w4 * 1024, i * 4096, voffV, srdV, so_v(n), i * 4096);
  };

  if (grpP) {
    f32x4 acc[7][4] = {};
    const Gemm<true> gm(lds0, w4);
    rawbar(); rawbar();
    const unsigned qa = lds0 + ((w4 >> 1) ? QI2 : QI1) + l15 * 256 + (l4 & 1) * 8 + (((8 * (w4 & 1) + (l4 >> 1)) ^ l15) << 4);
    gm.run(acc, QRING, [&](int m) {
      const f32x4 x = acc[m >> 2][m & 3] * QSCALE;
      *(LAS u32x2*)(size_t)((qa ^ ((m & 3) << 5)) + (m >> 2) * 4096) = u32x2{cvtpk(x[0], x[1]), cvtpk(x[2], x[3])};
    });
    bar();
    STAMP(0, 1); CSTAMP(0, 5);
  } else {
    u32x4 rk[6][8];
    const rsrc_t srdK = mkrsrc(kptr, KVBYTES);
    auto fetch = [&](auto RI) {
      constexpr int r = decltype(RI)::value;
      if constexpr (r < NR) {
#pragma unroll
        for (int i = 0; i < 4; ++i) ld16nt(rk[r][4 + i], voffK, srdK, so_k(RT[r]), i * 4096);
#pragma unroll
        for (int i = 0; i < 4; ++i) ld16nt(rk[r][i], voffV, srdV, so_v(RT[r]), i * 4096);
      }
    };
    auto put = [&](auto RI, unsigned slot) {
      constexpr int r = decltype(RI)::value;
      if constexpr (r < NR) {
#pragma unroll
        for (int i = 0; i < 8; ++i) *(LAS u32x4*)(size_t)(lds0 + slot + w4 * 1024 + lane * 16 + i * 4096) = rk[r][i];
      }
    };
    const GemmLoad<true> gl(srdX, srdWq, lds0, w4, hh, p);
    Ring r = QRING;
    gl.tile(0, r.b0, r.w0); rawbar();
    gl.tile(1, r.b1, r.w1);
    wait_bar<11>();
    gl.tile(2, r.b2, r.w2);
#pragma unroll
    for (int v = 0; v < 13; ++v) {
      constexpr unsigned BM = BMASK << 2;
      const int nb = ((BM >> v) & 1) + ((BM >> (v + 1)) & 1);
      if (nb == 0) wait_bar<11>(); else if (nb == 1) wait_bar<19>(); else wait_bar<27>();
      gl.tile(v + 3, r.b0, r.w0);
      if (v == RB[0]) fetch(IC(0)); if (v == RB[1]) fetch(IC(1)); if (v == RB[2]) fetch(IC(2));
      if (v == RB[3]) fetch(IC(3)); if (v == RB[4]) fetch(IC(4)); if (v == RB[5]) fetch(IC(5));
      r.rotate();
    }
    issue_k(-1, KVD);
    if constexpr ((BMASK >> 12) & 1) wait_bar<23>(); else wait_bar<15>();
    issue_kv(0, KVA);
    wait_bar<12>();
#pragma unroll
    for (int i = 0; i < NR; ++i) landed(rk[i]);
    rawbar();
    issue_kv(1, KVB);
    wait_bar<16>();
    STAMP(0, 1); CSTAMP(0, 5);
    wait_bar<8>();
    put(IC(0), KVC);
    unsigned ks = KVD, ks1 = KVA, ks2 = KVB, ks3 = KVC;
#pragma unroll
    for (int n = 0; n < 15; ++n) {
      const int u = n + 3;
      if ((RMASK >> u) & 1) {
        if (u == RT[1]) put(IC(1), ks); else if (u == RT[2]) put(IC(2), ks); else if (u == RT[3]) put(IC(3), ks);
        else if (u == RT[4]) put(IC(4), ks); else put(IC(5), ks);
      } else {
        if (SMOOTH) not_before(PA1 + n * PB1 + run_of(u) * SMOOTH);
        issue_kv(u, ks);
      }
      const unsigned t = ks; ks = ks1; ks1 = ks2; ks2 = ks3; ks3 = t;
      const int nd = 2 - ((RMASK >> (n + 2)) & 1) - ((RMASK >> u) & 1);
      if (nd == 2) wait_vm<16>(); else if (nd == 1) wait_vm<8>(); else wait_vm<0>();
      not_before(PA1 + (n + 1) * PB1);
      bar();
    }
  }

  u64 tag;
  asm volatile("s_load_dwordx2 %0, %1, 0x0" : "=&s"(tag) : "s"(epoch) : "memory");
  u32x4 of[8];
  {
    Attn at;
    at.init(lds0, w4, grpP ? QI1 : QI2, KVD, KVA);
    asm volatile("s_waitcnt lgkmcnt(0)" : "+s"(tag) :: "memory");
    tag += 1;
    if (!grpP) { STAMP(0, 2); CSTAMP(0, 6); }
    at.run();
    if (grpP) {
      STAMP(0, 2); CSTAMP(0, 6);
      bar();
      issue_kv(18, KVC);
    }
    at.finish(of);
  }
  const GemmLoad<false> gl(srdO, srdWo, lds0, w4, hh, p);

  if (grpP) {
    {
      unsigned ks = KVD, ks1 = KVA, ks2 = KVB, ks3 = KVC;
#pragma unroll 1
      for (unsigned n = 17; n < 30; ++n) {
        wait_vm<8>();
        not_before(PA2 + (n - 16) * PB2);
        bar();
        issue_kv(n + 2, ks);
        const unsigned t = ks; ks = ks1; ks1 = ks2; ks2 = ks3; ks3 = t;
      }
    }
    gl.w(0, ORING.w0);
    put_o<0>(of, lds0 + ORING.b0, w4);
    wait_bar<12>();
    put_o<1>(of, lds0 + ORING.b1, w4);
    publish_o(of, srdO, hh * 28672, w4);
    wait_bar<12>();
    gl.w(1, ORING.w1); gl.w(2, ORING.w2);
    wait_vm<8>();
    if (lane == 0) st_flag(fam + (hh * 4 + w4) * 64, tag);
    f32x4 acc[7][4] = {};
    const Gemm<false> gm(lds0, w4);
    rawbar();
    STAMP(1, 0);
    wait_bar<0>();
    STAMP(1, 2);
    const unsigned bid0 = bid & ~6u, xo = bid0 * 65536 + g * 16384 + w4 * 4096 + lane * 16;
    gm.run(acc, ORING, [&](int m) {
      const int j = m >> 2, n = m & 3;
      if ((n & 1) && (unsigned)(j >> 1) != g) {
        const f32x4 a = acc[j][n - 1], b = acc[j][n];
        const u32x4 w = {cvtpk(a[0], a[1]), cvtpk(a[2], a[3]), cvtpk(b[0], b[1]), cvtpk(b[2], b[3])};
        __builtin_amdgcn_raw_buffer_store_b128(w, srdE, xo + (j >> 1) * 131072 + ((j & 1) * 2 + (n >> 1)) * 1024, 0, 16);
      }
    });
    STAMP(1, 3);
    f32x4 t[2][4];
#pragma unroll
    for (int n = 0; n < 4; ++n) {
      t[0][n] = g == 0 ? acc[0][n] : g == 1 ? acc[2][n] : g == 2 ? acc[4][n] : acc[6][n];
      t[1][n] = g == 0 ? acc[1][n] : g == 1 ? acc[3][n] : acc[5][n];
    }
    wait_vm<0>();
    STAMP(1, 4);
    if (lane < 4) st_flag(ws + WS_G + (((bid0 + 2 * lane) * 4 + w4) * 4 + g) * 64, tag);
    poll(ws + WS_G + ((bid * 4 + w4) * 4 + (lane & 3)) * 64, lane < 4, tag);
    u32x4 e[3][2][2];
#pragma unroll
    for (int jj = 0; jj < 2; ++jj)
#pragma unroll
      for (int n = 0; n < 2; ++n)
#pragma unroll
        for (unsigned sr = 0; sr < 3; ++sr)
          e[sr][jj][n] = __builtin_amdgcn_raw_buffer_load_b128(srdE, (jj && g == 3) ? 0x80000000u : lane * 16 + (jj * 2 + n) * 1024, bid * 65536 + ((g + sr + 1) & 3) * 16384 + w4 * 4096, 16);
    const unsigned yo = ((w4 >> 1) * SQ + 32 * g + l15) * 4096 + (w4 & 1) * 128 + (l4 & 1) * 32 + (l4 >> 1) * 16;
#pragma unroll
    for (int jj = 0; jj < 2; ++jj)
#pragma unroll
      for (int n = 0; n < 2; ++n) {
        f32x4 a = t[jj][2 * n], b = t[jj][2 * n + 1];
#pragma unroll
        for (int sr = 0; sr < 3; ++sr) {
          const u32x4 x = e[sr][jj][n];
          a += f32x4{__uint_as_float(x[0] << 16), __uint_as_float(x[0] & 0xffff0000u), __uint_as_float(x[1] << 16), __uint_as_float(x[1] & 0xffff0000u)};
          b += f32x4{__uint_as_float(x[2] << 16), __uint_as_float(x[2] & 0xffff0000u), __uint_as_float(x[3] << 16), __uint_as_float(x[3] & 0xffff0000u)};
        }
        const auto lo = __builtin_amdgcn_permlane16_swap(cvtpk(a[0], a[1]), cvtpk(b[0], b[1]), false, false);
        const auto hi = __builtin_amdgcn_permlane16_swap(cvtpk(a[2], a[3]), cvtpk(b[2], b[3]), false, false);
        __builtin_amdgcn_raw_buffer_store_b128(u32x4{lo[0], hi[0], lo[1], hi[1]}, srdY, (jj && g == 3) ? 0x80000000u : yo + jj * 65536 + n * 64, 0, YAUX);
      }
    if (g == 0 && hh == 0 && wid == 0 && lane == 0) st_flag(epoch, tag);
  } else {
    publish_o(of, srdO, (32 + hh) * 28672, w4);
    STAMP(1, 0);
    rawbar();
    put_o<0>(of, lds0 + ORING.b0 + 14336, w4); put_o<1>(of, lds0 + ORING.b1 + 14336, w4);
    bar();
    wait_vm<0>();
    if (lane == 0) st_flag(fam + 2048 + (hh * 4 + w4) * 64, tag);
    rawbar();
    Ring r = ORING;
    gl.w(3, r.w0);
    gl.w(4, 102400);
    gl.w(5, 102400);
    STAMP(1, 1);
    poll(fam + lane * 64, true, tag);
    STAMP(1, 2);
    gl.b(2, r.b2); gl.b(3, r.b0);
    wait_bar<7>();
    r.rotate();
    gl.tile(4, r.b0, r.w0);
    r.rotate();
#pragma unroll 1
    for (unsigned v = 2; v < 13; ++v) { wait_bar<11>(); gl.tile(v + 3, r.b0, r.w0); r.rotate(); }
    wait_bar<11>();
    wait_bar<0>();
    rawbar();
    STAMP(1, 3); STAMP(1, 4);
  }
    STAMP(1, 5); STAMP(1, 6); STAMP(1, 7); STAMP(0, 3);
}

extern "C" void kernel_launch(void* const* d_in, const int* in_sizes, int n_in, void* d_out, int out_size, void* d_ws, size_t ws_size, hipStream_t stream) {
  (void)hipFuncSetAttribute((const void*)k_fused, hipFuncAttributeMaxDynamicSharedMemorySize, 163840);
  k_fused<<<256, 512, 163840, stream>>>((const u16*)d_in[0], (const u16*)d_in[1], (const u16*)d_in[2], (const u16*)d_in[3], (const u16*)d_in[4],
                                        (const u16*)d_in[5], (const u16*)d_in[6], (const u16*)d_in[7], (const u16*)d_in[8], (const u16*)d_in[9],
                                        (char*)d_ws, (u16*)d_out);
}
```
